```python
import torch

def _softmax(x, axis=-1):
    return torch.softmax(x, dim=axis)
def _gelu(x):
    return torch.nn.functional.gelu(x, approximate="tanh")
def _silu(x):
    return torch.nn.functional.silu(x)
def _sigmoid(x):
    return torch.sigmoid(x)
def _layernorm(x, axis=-1, eps=1e-5):
    m = x.mean(dim=axis, keepdim=True)
    v = x.var(dim=axis, keepdim=True, unbiased=False)
    return (x - m) / torch.sqrt(v + eps)
def _scatter_add(tgt, idx, src, axis=0):
    return tgt.clone().index_add_(axis, idx, src)
def _segment_sum(src, seg, n):
    out = torch.zeros((n,) + src.shape[1:], dtype=src.dtype)
    return out.index_add_(0, seg, src)
def _pad(x, pw):
    flat = tuple(v for pair in reversed(pw) for v in pair)
    return torch.nn.functional.pad(x, flat)

kernel_name = 'parallel_sum__gqa_block_cached__geglu_ffn__decode'

def setup_inputs():

    _w = (lambda a: (lambda t: t.to(torch.bfloat16) if t.is_floating_point() else t)(torch.as_tensor(a)))
    def _A_setup():
        B, Sq, D = 2, 1, 448
        Hq, Hkv, Sk, Dh = 16, 8, 32768, 256
        assert Hq % Hkv == 0, (Hq, Hkv)
        return {
            "x": _w((lambda *s: torch.randn(*s, dtype=torch.bfloat16) * 0.05)(B, Sq, D)),
            "k_cache": _w((lambda *s: torch.randn(*s, dtype=torch.bfloat16) * 3.2)(B, Hkv, Sk, Dh)),
            "v_cache": _w((lambda *s: torch.randn(*s, dtype=torch.bfloat16) * 1.6)(B, Hkv, Sk, Dh)),
            "wq": _w((lambda *s: torch.randn(*s, dtype=torch.bfloat16) * 3.2)(D, Hq * Dh) * (D ** -0.5)),
            "wo": _w((lambda *s: torch.randn(*s, dtype=torch.bfloat16) * 1.6)(Hq * Dh, D) * ((Hq * Dh) ** -0.5)),
        }
    _A_in = _A_setup()

    def _B_setup():
        B, S, D, F = 2, 1, 448, 1792
        return {
            "x": _w((lambda *s: torch.randn(*s, dtype=torch.bfloat16) * 0.05)(B, S, D)),
            "w_gate": _w((lambda *s: torch.randn(*s, dtype=torch.bfloat16) * 0.05)(D, F)),
            "w_up": _w((lambda *s: torch.randn(*s, dtype=torch.bfloat16) * 0.05)(D, F)),
            "w_down": _w((lambda *s: torch.randn(*s, dtype=torch.bfloat16) * 0.05)(F, D)),
        }
    _B_in = _B_setup()

    return {
        "A_x": _A_in["x"],
        "A_k_cache": _A_in["k_cache"],
        "A_v_cache": _A_in["v_cache"],
        "A_wq": _A_in["wq"],
        "A_wo": _A_in["wo"],
        "B_x": _B_in["x"],
        "B_w_gate": _B_in["w_gate"],
        "B_w_up": _B_in["w_up"],
        "B_w_down": _B_in["w_down"],
    }

def reference(A_x, A_k_cache, A_v_cache, A_wq, A_wo, B_x, B_w_gate, B_w_up, B_w_down):
    def _A_ref(x, k_cache, v_cache, wq, wo):
        B, Sq, D = x.shape
        Hq, Dh = 16, 256
        Hkv = k_cache.shape[1]
        rep = Hq // Hkv
        q = (lambda x, *d: x.permute(*d))((x @ wq).reshape(B, Sq, Hq, Dh), 0, 2, 1, 3)
        k = (lambda x, n, axis: torch.repeat_interleave(x, n, dim=axis))(k_cache, rep, axis=1)
        v = (lambda x, n, axis: torch.repeat_interleave(x, n, dim=axis))(v_cache, rep, axis=1)
        scores = torch.einsum("bhqd,bhkd->bhqk", q, k) * (Dh ** -0.5)
        p = _softmax(scores, axis=-1)
        o = torch.einsum("bhqk,bhkd->bhqd", p, v)
        return (lambda x, *d: x.permute(*d))(o, 0, 2, 1, 3).reshape(B, Sq, Hq * Dh) @ wo

    def _B_ref(x, w_gate, w_up, w_down):
        return (_gelu(x @ w_gate) * (x @ w_up)) @ w_down

    x = A_x
    xn = _layernorm(x, axis=-1)
    return x + _A_ref(xn, A_k_cache, A_v_cache, A_wq, A_wo) + _B_ref(xn, B_w_gate, B_w_up, B_w_down)
```

```cpp
#include <hip/hip_runtime.h>
#include <hip/hip_bf16.h>
#include <stdint.h>


constexpr int NB = 2, DM = 448, HQ = 16, HKV = 8, SK = 32768, DH = 256, FF = 1792;
constexpr int NSPLIT = 16;
constexpr int KEYS_WG = SK / NSPLIT;
constexpr int ATT_WAVES = 8;
constexpr int KEYS_WAVE = KEYS_WG / ATT_WAVES;
constexpr int CHUNK = 32;
constexpr int NCHUNK = KEYS_WAVE / CHUNK;

constexpr size_t WS_CNT = 0;
constexpr size_t WS_Q = 1024;
constexpr size_t WS_H = WS_Q + NB * HQ * DH * 2;
constexpr size_t WS_PM = WS_H + 8192;
constexpr size_t WS_PL = WS_PM + NB * HQ * NSPLIT * 4;
constexpr size_t WS_PO = WS_PL + NB * HQ * NSPLIT * 4;
constexpr size_t WS_PART = WS_PO + (size_t)NB * HQ * NSPLIT * DH * 4;

using u32x4 = __attribute__((ext_vector_type(4))) unsigned;
using f32x4 = __attribute__((ext_vector_type(4))) float;
using bf16x2 = __attribute__((ext_vector_type(2))) __bf16;

__device__ __forceinline__ float bf2f(unsigned short u) { return __uint_as_float(((unsigned)u) << 16); }
__device__ __forceinline__ float bflo(unsigned u) { return __uint_as_float(u << 16); }
__device__ __forceinline__ float bfhi(unsigned u) { return __uint_as_float(u & 0xffff0000u); }
__device__ __forceinline__ unsigned short f2bf(float x) { __bf16 h = (__bf16)x; return __builtin_bit_cast(unsigned short, h); }
__device__ __forceinline__ float bfr(float x) { __bf16 h = (__bf16)x; return (float)h; }
__device__ __forceinline__ float dot2(unsigned a, unsigned b, float acc) {
  return __builtin_amdgcn_fdot2_f32_bf16(__builtin_bit_cast(bf16x2, a), __builtin_bit_cast(bf16x2, b), acc, false);
}
template <int CTRL> __device__ __forceinline__ float dpp(float x) {
  return __builtin_bit_cast(float, __builtin_amdgcn_mov_dpp(__builtin_bit_cast(int, x), CTRL, 0xf, 0xf, true));
}
constexpr int XOR1 = 0xB1, XOR2 = 0x4E, XOR7 = 0x141, XOR8 = 0x128;

__device__ __forceinline__ float wave_sum(float v) {
#pragma unroll
  for (int o = 32; o > 0; o >>= 1) v += __shfl_xor(v, o, 64);
  return v;
}

__device__ __forceinline__ void layernorm_to_lds(const unsigned short* __restrict__ x, float* xn) {
  const int t = threadIdx.x;
  if (t < 128) {
    const int b = t >> 6, lane = t & 63;
    float v[7];
    float s = 0.f;
#pragma unroll
    for (int i = 0; i < 7; ++i) { v[i] = bf2f(x[b * DM + lane + 64 * i]); s += v[i]; }
    s = wave_sum(s);
    const float mean = s * (1.0f / DM);
    float ss = 0.f;
#pragma unroll
    for (int i = 0; i < 7; ++i) { const float d = v[i] - mean; ss += d * d; }
    ss = wave_sum(ss);
    const float var = ss * (1.0f / DM);
    const float mb = bfr(mean), vb = bfr(var);
    const float den = bfr(sqrtf(bfr(vb + 1e-5f)));
#pragma unroll
    for (int i = 0; i < 7; ++i) xn[b * DM + lane + 64 * i] = bfr(bfr(v[i] - mb) / den);
  }
}

constexpr int PREP_Q_WGS = HQ * DH / 32;
constexpr int PREP_F_WGS = FF / 32;

__device__ __forceinline__ void gemv32_acc(const unsigned short* __restrict__ W, int ldw, int c0, const float* xn,
                                           float (&acc)[2][8]) {
  const int t = threadIdx.x, cq = t & 3, rg = t >> 2;
  u32x4 w[7];
#pragma unroll
  for (int i = 0; i < 7; ++i)
    w[i] = *reinterpret_cast<const u32x4*>(W + (size_t)(rg + 64 * i) * ldw + c0 + cq * 8);
#pragma unroll
  for (int b = 0; b < 2; ++b)
#pragma unroll
    for (int k = 0; k < 8; ++k) acc[b][k] = 0.f;
#pragma unroll
  for (int i = 0; i < 7; ++i) {
    const float x0 = xn[rg + 64 * i], x1 = xn[DM + rg + 64 * i];
#pragma unroll
    for (int k = 0; k < 4; ++k) {
      const float lo = bflo(w[i][k]), hi = bfhi(w[i][k]);
      acc[0][2 * k] = fmaf(x0, lo, acc[0][2 * k]); acc[0][2 * k + 1] = fmaf(x0, hi, acc[0][2 * k + 1]);
      acc[1][2 * k] = fmaf(x1, lo, acc[1][2 * k]); acc[1][2 * k + 1] = fmaf(x1, hi, acc[1][2 * k + 1]);
    }
  }
#pragma unroll
  for (int b = 0; b < 2; ++b)
#pragma unroll
    for (int k = 0; k < 8; ++k) {
      float v = acc[b][k];
      v += __shfl_xor(v, 4, 64); v += __shfl_xor(v, 8, 64); v += __shfl_xor(v, 16, 64); v += __shfl_xor(v, 32, 64);
      acc[b][k] = v;
    }
}

__device__ __forceinline__ float gelu_tanh(float g) {
  const float u = 0.7978845608028654f * (g + 0.044715f * g * g * g);
  return 0.5f * g * (1.0f + tanhf(u));
}

__global__ __launch_bounds__(256) void prep_kernel(const unsigned short* __restrict__ x,
                                                   const unsigned short* __restrict__ wq,
                                                   const unsigned short* __restrict__ wg,
                                                   const unsigned short* __restrict__ wu,
                                                   unsigned char* __restrict__ ws) {
  __shared__ float lds[2 * DM + 2 * 4 * 2 * 32];
  float* xn = lds;
  float* red = lds + 2 * DM;
  const int t = threadIdx.x, wave = t >> 6, lane = t & 63;
  if (blockIdx.x == 0 && t < 8) reinterpret_cast<unsigned*>(ws + WS_CNT)[t * 16] = 0u;
  layernorm_to_lds(x, xn);
  __syncthreads();
  if (blockIdx.x < PREP_Q_WGS) {
    const int c0 = blockIdx.x * 32;
    float acc[2][8];
    gemv32_acc(wq, HQ * DH, c0, xn, acc);
    if (lane < 4) {
#pragma unroll
      for (int b = 0; b < 2; ++b)
#pragma unroll
        for (int k = 0; k < 8; ++k) red[(wave * 2 + b) * 32 + lane * 8 + k] = acc[b][k];
    }
    __syncthreads();
    if (t < 64) {
      const int b = t >> 5, c = t & 31;
      const float v = red[(0 * 2 + b) * 32 + c] + red[(1 * 2 + b) * 32 + c] + red[(2 * 2 + b) * 32 + c] + red[(3 * 2 + b) * 32 + c];
      reinterpret_cast<unsigned short*>(ws + WS_Q)[b * (HQ * DH) + c0 + c] = f2bf(v);
    }
  } else {
    const int c0 = (blockIdx.x - PREP_Q_WGS) * 32;
    float accg[2][8], accu[2][8];
    gemv32_acc(wg, FF, c0, xn, accg);
    gemv32_acc(wu, FF, c0, xn, accu);
    if (lane < 4) {
#pragma unroll
      for (int b = 0; b < 2; ++b)
#pragma unroll
        for (int k = 0; k < 8; ++k) {
          red[(wave * 2 + b) * 32 + lane * 8 + k] = accg[b][k];
          red[256 + (wave * 2 + b) * 32 + lane * 8 + k] = accu[b][k];
        }
    }
    __syncthreads();
    if (t < 64) {
      const int b = t >> 5, c = t & 31;
      const float g = bfr(red[(0 * 2 + b) * 32 + c] + red[(1 * 2 + b) * 32 + c] + red[(2 * 2 + b) * 32 + c] + red[(3 * 2 + b) * 32 + c]);
      const float u = bfr(red[256 + (0 * 2 + b) * 32 + c] + red[256 + (1 * 2 + b) * 32 + c] + red[256 + (2 * 2 + b) * 32 + c] + red[256 + (3 * 2 + b) * 32 + c]);
      reinterpret_cast<unsigned short*>(ws + WS_H)[b * FF + c0 + c] = f2bf(bfr(gelu_tanh(g)) * u);
    }
  }
}

__device__ __forceinline__ u32x4 ldnt(const u32x4* p) { return __builtin_nontemporal_load(p); }

#define PIN16(a) asm volatile("" : "+v"(a[0]), "+v"(a[1]), "+v"(a[2]), "+v"(a[3]), "+v"(a[4]), "+v"(a[5]), "+v"(a[6]), "+v"(a[7]), \
    "+v"(a[8]), "+v"(a[9]), "+v"(a[10]), "+v"(a[11]), "+v"(a[12]), "+v"(a[13]), "+v"(a[14]), "+v"(a[15]) :: "memory")

template <bool PF>
__device__ __forceinline__ void chunk_body(u32x4 (&kb)[16], u32x4 (&vb)[16], const u32x4* kp, const u32x4* vp,
                                           const u32x4& q0, const u32x4& q1, float (&acc)[16], float& m, float& l,
                                           float* pw, int lane) {
  constexpr float SC = 0.0625f * 1.4426950408889634f;
  float x[16];
#pragma unroll
  for (int i = 0; i < 16; ++i) {
    float s0 = 0.f, s1 = 0.f;
#pragma unroll
    for (int k = 0; k < 4; ++k) { s0 = dot2(kb[i][k], q0[k], s0); s1 = dot2(kb[i][k], q1[k], s1); }
    auto r = __builtin_amdgcn_permlane16_swap(__float_as_uint(s0), __float_as_uint(s1), false, false);
    x[i] = __uint_as_float(r[0]) + __uint_as_float(r[1]);
  }
  PIN16(x);
  if (PF) {
#pragma unroll
    for (int i = 0; i < 16; ++i) kb[i] = ldnt(kp + i * 64);
  }
  asm volatile("" ::: "memory");
#define TR_STEP(HALF, CTRL, BIT)                                                   \
  _Pragma("unroll") for (int h = 0; h < (HALF); ++h) {                             \
    const bool up = (lane & (BIT)) != 0;                                           \
    const float keep = up ? x[h + (HALF)] : x[h], send = up ? x[h] : x[h + (HALF)]; \
    x[h] = keep + dpp<CTRL>(send);                                                 \
  }
  TR_STEP(8, XOR8, 8) TR_STEP(4, XOR7, 4) TR_STEP(2, XOR2, 2) TR_STEP(1, XOR1, 1)
#undef TR_STEP
  const float s = bfr(x[0]) * SC;
  float pm = s;
  pm = fmaxf(pm, dpp<XOR1>(pm)); pm = fmaxf(pm, dpp<XOR2>(pm)); pm = fmaxf(pm, dpp<XOR7>(pm)); pm = fmaxf(pm, dpp<XOR8>(pm));
  {
    auto r = __builtin_amdgcn_permlane32_swap(__float_as_uint(pm), __float_as_uint(pm), false, false);
    pm = fmaxf(__uint_as_float(r[0]), __uint_as_float(r[1]));
  }
  const float mn = fmaxf(m, pm);
  const float alpha = __builtin_amdgcn_exp2f(m - mn);
  const float p = __builtin_amdgcn_exp2f(s - mn);
  l = fmaf(l, alpha, p);
  m = mn;
  const float a0 = __uint_as_float(__builtin_amdgcn_readlane(__float_as_uint(alpha), 0));
  const float a1 = __uint_as_float(__builtin_amdgcn_readlane(__float_as_uint(alpha), 16));
#pragma unroll
  for (int k = 0; k < 8; ++k) { acc[k] *= a0; acc[8 + k] *= a1; }
  pw[lane] = p;
  asm volatile("s_waitcnt lgkmcnt(0)" ::: "memory");
  const float* prd = pw + (lane >> 5) * 32;
#pragma unroll
  for (int u = 0; u < 4; ++u) {
    const f32x4 pa = *reinterpret_cast<const f32x4*>(prd + 4 * u);
    const f32x4 pb = *reinterpret_cast<const f32x4*>(prd + 16 + 4 * u);
    asm volatile("" : "+v"(vb[4 * u]), "+v"(vb[4 * u + 1]), "+v"(vb[4 * u + 2]), "+v"(vb[4 * u + 3]));
#pragma unroll
    for (int j = 0; j < 4; ++j) {
      const int i = 4 * u + j;
      const float p0 = pa[j], p1 = pb[j];
#pragma unroll
      for (int k = 0; k < 4; ++k) {
        const float lo = bflo(vb[i][k]), hi = bfhi(vb[i][k]);
        acc[2 * k] = fmaf(p0, lo, acc[2 * k]); acc[2 * k + 1] = fmaf(p0, hi, acc[2 * k + 1]);
        acc[8 + 2 * k] = fmaf(p1, lo, acc[8 + 2 * k]); acc[8 + 2 * k + 1] = fmaf(p1, hi, acc[8 + 2 * k + 1]);
      }
    }
  }
  PIN16(acc);
  if (PF) {
#pragma unroll
    for (int i = 0; i < 16; ++i) vb[i] = ldnt(vp + i * 64);
  }
  asm volatile("" ::: "memory");
}

__global__ __launch_bounds__(512, 2) void attn_kernel(const u32x4* __restrict__ K, const u32x4* __restrict__ V,
                                                      unsigned char* __restrict__ ws) {
  __shared__ __attribute__((aligned(16))) float lds[ATT_WAVES * 64 + ATT_WAVES * 2 * DH + ATT_WAVES * 4];
  float* plds = lds;
  float* mrg = lds + ATT_WAVES * 64;
  float* mls = mrg + ATT_WAVES * 2 * DH;
  const int t = threadIdx.x, lane = t & 63, wave = __builtin_amdgcn_readfirstlane(t >> 6);
  const int kvh = blockIdx.x & 7, rest = blockIdx.x >> 3, b = rest & 1, split = rest >> 1;
  const int bh = b * HKV + kvh;
  const int c = lane & 31;
  const size_t base = ((size_t)bh * SK + (size_t)split * KEYS_WG + (size_t)wave * KEYS_WAVE) * (DH / 8) + lane;
  const u32x4* kp = K + base;
  const u32x4* vp = V + base;
  const u32x4* qg = reinterpret_cast<const u32x4*>(ws + WS_Q) + ((size_t)b * HQ + kvh * 2) * (DH / 8);
  const u32x4 q0 = qg[c], q1 = qg[32 + c];
  u32x4 kb[16], vb[16];
  asm volatile("" ::: "memory");
#pragma unroll
  for (int i = 0; i < 16; ++i) kb[i] = ldnt(kp + i * 64);
  asm volatile("" ::: "memory");
#pragma unroll
  for (int i = 0; i < 16; ++i) vb[i] = ldnt(vp + i * 64);
  asm volatile("" ::: "memory");
  float acc[16];
#pragma unroll
  for (int k = 0; k < 16; ++k) acc[k] = 0.f;
  float m = -INFINITY, l = 0.f;
  float* pw = plds + wave * 64;
  for (int ch = 0; ch < NCHUNK - 1; ++ch) {
    kp += 1024; vp += 1024;
    chunk_body<true>(kb, vb, kp, vp, q0, q1, acc, m, l, pw, lane);
  }
  chunk_body<false>(kb, vb, kp, vp, q0, q1, acc, m, l, pw, lane);
#pragma unroll
  for (int k = 0; k < 16; ++k) {
    auto r = __builtin_amdgcn_permlane32_swap(__float_as_uint(acc[k]), __float_as_uint(acc[k]), false, false);
    acc[k] = __uint_as_float(r[0]) + __uint_as_float(r[1]);
  }
  l += dpp<XOR1>(l); l += dpp<XOR2>(l); l += dpp<XOR7>(l); l += dpp<XOR8>(l);
  {
    auto r = __builtin_amdgcn_permlane32_swap(__float_as_uint(l), __float_as_uint(l), false, false);
    l = __uint_as_float(r[0]) + __uint_as_float(r[1]);
  }
  if (lane < 32) {
#pragma unroll
    for (int h = 0; h < 2; ++h) {
      *reinterpret_cast<f32x4*>(mrg + (wave * 2 + h) * DH + 8 * c) = f32x4{acc[8 * h + 0], acc[8 * h + 1], acc[8 * h + 2], acc[8 * h + 3]};
      *reinterpret_cast<f32x4*>(mrg + (wave * 2 + h) * DH + 8 * c + 4) = f32x4{acc[8 * h + 4], acc[8 * h + 5], acc[8 * h + 6], acc[8 * h + 7]};
    }
    if ((lane & 15) == 0) { const int h = lane >> 4; mls[(wave * 2 + h) * 2] = m; mls[(wave * 2 + h) * 2 + 1] = l; }
  }
  __syncthreads();
  {
    const int h = t >> 8, d = t & 255;
    float M = -INFINITY;
#pragma unroll
    for (int w = 0; w < ATT_WAVES; ++w) M = fmaxf(M, mls[(w * 2 + h) * 2]);
    float L = 0.f, o = 0.f;
#pragma unroll
    for (int w = 0; w < ATT_WAVES; ++w) {
      const float wt = __builtin_amdgcn_exp2f(mls[(w * 2 + h) * 2] - M);
      L = fmaf(wt, mls[(w * 2 + h) * 2 + 1], L);
      o = fmaf(wt, mrg[(w * 2 + h) * DH + d], o);
    }
    const int row = (b * HQ + kvh * 2 + h) * NSPLIT + split;
    reinterpret_cast<float*>(ws + WS_PO)[(size_t)row * DH + d] = o;
    if (d == 0) { reinterpret_cast<float*>(ws + WS_PM)[row] = M; reinterpret_cast<float*>(ws + WS_PL)[row] = L; }
  }
}

constexpr int KP_O = HQ * DH / 128;
constexpr int KP_D = FF / 128;
constexpr int KP = KP_O + KP_D;
constexpr int CG = DM / 64;

__global__ __launch_bounds__(256) void tail_kernel(const unsigned short* __restrict__ x,
                                                   const unsigned short* __restrict__ wo,
                                                   const unsigned short* __restrict__ wd,
                                                   unsigned char* __restrict__ ws, unsigned short* __restrict__ out) {
  __shared__ float lds[2 * 128 + 4 * 2 * 64 + 4];
  float* vec = lds;
  float* red = lds + 256;
  unsigned* flag = reinterpret_cast<unsigned*>(lds + 256 + 512);
  const int t = threadIdx.x, lane = t & 63, wave = t >> 6;
  const int cg = blockIdx.x % CG, kp = blockIdx.x / CG;
  const int cq = t & 7, rg = t >> 3;
  const unsigned short* W = kp < KP_O ? wo + (size_t)kp * 128 * DM : wd + (size_t)(kp - KP_O) * 128 * DM;
  u32x4 w[4];
#pragma unroll
  for (int i = 0; i < 4; ++i) w[i] = *reinterpret_cast<const u32x4*>(W + (size_t)(rg + 32 * i) * DM + cg * 64 + cq * 8);
  {
    const int b = t >> 7, d = t & 127;
    if (kp < KP_O) {
      const int head = kp >> 1, dh = kp & 1;
      const int row0 = (b * HQ + head) * NSPLIT;
      const float* pm = reinterpret_cast<const float*>(ws + WS_PM) + row0;
      const float* pl = reinterpret_cast<const float*>(ws + WS_PL) + row0;
      const float* po = reinterpret_cast<const float*>(ws + WS_PO) + (size_t)row0 * DH + dh * 128 + d;
      float ms[NSPLIT], ls[NSPLIT], os[NSPLIT];
#pragma unroll
      for (int s = 0; s < NSPLIT; ++s) { ms[s] = pm[s]; ls[s] = pl[s]; os[s] = po[(size_t)s * DH]; }
      float M = -INFINITY;
#pragma unroll
      for (int s = 0; s < NSPLIT; ++s) M = fmaxf(M, ms[s]);
      float L = 0.f, o = 0.f;
#pragma unroll
      for (int s = 0; s < NSPLIT; ++s) { const float wt = __builtin_amdgcn_exp2f(ms[s] - M); L = fmaf(wt, ls[s], L); o = fmaf(wt, os[s], o); }
      vec[b * 128 + d] = bfr(o / L);
    } else {
      vec[b * 128 + d] = bf2f(reinterpret_cast<const unsigned short*>(ws + WS_H)[b * FF + (kp - KP_O) * 128 + d]);
    }
  }
  __syncthreads();
  float acc[2][8];
#pragma unroll
  for (int b = 0; b < 2; ++b)
#pragma unroll
    for (int k = 0; k < 8; ++k) acc[b][k] = 0.f;
#pragma unroll
  for (int i = 0; i < 4; ++i) {
    const float x0 = vec[rg + 32 * i], x1 = vec[128 + rg + 32 * i];
#pragma unroll
    for (int k = 0; k < 4; ++k) {
      const float lo = bflo(w[i][k]), hi = bfhi(w[i][k]);
      acc[0][2 * k] = fmaf(x0, lo, acc[0][2 * k]); acc[0][2 * k + 1] = fmaf(x0, hi, acc[0][2 * k + 1]);
      acc[1][2 * k] = fmaf(x1, lo, acc[1][2 * k]); acc[1][2 * k + 1] = fmaf(x1, hi, acc[1][2 * k + 1]);
    }
  }
#pragma unroll
  for (int b = 0; b < 2; ++b)
#pragma unroll
    for (int k = 0; k < 8; ++k) {
      float v = acc[b][k];
      v += __shfl_xor(v, 8, 64); v += __shfl_xor(v, 16, 64); v += __shfl_xor(v, 32, 64);
      acc[b][k] = v;
    }
  if (lane < 8) {
#pragma unroll
    for (int b = 0; b < 2; ++b)
#pragma unroll
      for (int k = 0; k < 8; ++k) red[(wave * 2 + b) * 64 + lane * 8 + k] = acc[b][k];
  }
  __syncthreads();
  float* part = reinterpret_cast<float*>(ws + WS_PART) + (size_t)cg * KP * 128;
  if (t < 128) {
    const int b = t >> 6, cc = t & 63;
    const float v = red[(0 * 2 + b) * 64 + cc] + red[(1 * 2 + b) * 64 + cc] + red[(2 * 2 + b) * 64 + cc] + red[(3 * 2 + b) * 64 + cc];
    __hip_atomic_store(part + kp * 128 + t, v, __ATOMIC_RELAXED, __HIP_MEMORY_SCOPE_AGENT);
  }
  asm volatile("s_waitcnt vmcnt(0)" ::: "memory");
  __syncthreads();
  if (t == 0) {
    unsigned* cnt = reinterpret_cast<unsigned*>(ws + WS_CNT) + cg * 16;
    const unsigned old = __hip_atomic_fetch_add(cnt, 1u, __ATOMIC_RELAXED, __HIP_MEMORY_SCOPE_AGENT);
    const unsigned last = (old == (unsigned)(KP - 1)) ? 1u : 0u;
    if (last) { __builtin_amdgcn_fence(__ATOMIC_ACQUIRE, "agent"); asm volatile("s_waitcnt vmcnt(0)" ::: "memory"); }
    flag[0] = last;
  }
  __syncthreads();
  if (flag[0] == 0u) return;
  if (t < 128) {
    const int b = t >> 6, cc = t & 63;
    float v[KP];
#pragma unroll
    for (int k = 0; k < KP; ++k) v[k] = __hip_atomic_load(part + k * 128 + t, __ATOMIC_RELAXED, __HIP_MEMORY_SCOPE_AGENT);
    float A = 0.f, Bf = 0.f;
#pragma unroll
    for (int k = 0; k < KP_O; ++k) A += v[k];
#pragma unroll
    for (int k = KP_O; k < KP; ++k) Bf += v[k];
    const float xv = bf2f(x[b * DM + cg * 64 + cc]);
    out[b * DM + cg * 64 + cc] = f2bf(bfr(xv + bfr(A)) + bfr(Bf));
  }
}

extern "C" void kernel_launch(void* const* d_in, const int* in_sizes, int n_in, void* d_out, int out_size, void* d_ws,
                              size_t ws_size, hipStream_t stream) {
  const unsigned short* x = (const unsigned short*)d_in[0];
  const u32x4* K = (const u32x4*)d_in[1];
  const u32x4* V = (const u32x4*)d_in[2];
  const unsigned short* wq = (const unsigned short*)d_in[3];
  const unsigned short* wo = (const unsigned short*)d_in[4];
  const unsigned short* wg = (const unsigned short*)d_in[6];
  const unsigned short* wu = (const unsigned short*)d_in[7];
  const unsigned short* wd = (const unsigned short*)d_in[8];
  unsigned char* ws = (unsigned char*)d_ws;
  prep_kernel<<<PREP_Q_WGS + PREP_F_WGS, 256, 0, stream>>>(x, wq, wg, wu, ws);
  attn_kernel<<<NB * HKV * NSPLIT, 512, 0, stream>>>(K, V, ws);
  tail_kernel<<<KP * CG, 256, 0, stream>>>(x, wo, wd, ws, (unsigned short*)d_out);
}
```

```cpp
#include <hip/hip_runtime.h>
#include <hip/hip_bf16.h>
#include <stdint.h>


constexpr int NB = 2, DM = 448, HQ = 16, HKV = 8, SK = 32768, DH = 256, FF = 1792;
constexpr int NSPLIT = 16;
constexpr int KEYS_WG = SK / NSPLIT;
constexpr int ATT_WAVES = 8;
constexpr int KEYS_WAVE = KEYS_WG / ATT_WAVES;
constexpr int CHUNK = 32;
constexpr int NCHUNK = KEYS_WAVE / CHUNK;

constexpr size_t WS_CNT = 0;
constexpr size_t WS_Q = 1024;
constexpr size_t WS_H = WS_Q + NB * HQ * DH * 2;
constexpr size_t WS_PM = WS_H + 8192;
constexpr size_t WS_PL = WS_PM + NB * HQ * NSPLIT * 4;
constexpr size_t WS_PO = WS_PL + NB * HQ * NSPLIT * 4;
constexpr size_t WS_PART = WS_PO + (size_t)NB * HQ * NSPLIT * DH * 4;

using u32x4 = __attribute__((ext_vector_type(4))) unsigned;
using f32x4 = __attribute__((ext_vector_type(4))) float;
using bf16x2 = __attribute__((ext_vector_type(2))) __bf16;

__device__ __forceinline__ float bf2f(unsigned short u) { return __uint_as_float(((unsigned)u) << 16); }
__device__ __forceinline__ float bflo(unsigned u) { return __uint_as_float(u << 16); }
__device__ __forceinline__ float bfhi(unsigned u) { return __uint_as_float(u & 0xffff0000u); }
__device__ __forceinline__ unsigned short f2bf(float x) { __bf16 h = (__bf16)x; return __builtin_bit_cast(unsigned short, h); }
__device__ __forceinline__ float bfr(float x) { __bf16 h = (__bf16)x; return (float)h; }
__device__ __forceinline__ float dot2(unsigned a, unsigned b, float acc) {
  return __builtin_amdgcn_fdot2_f32_bf16(__builtin_bit_cast(bf16x2, a), __builtin_bit_cast(bf16x2, b), acc, false);
}
template <int CTRL> __device__ __forceinline__ float dpp(float x) {
  return __builtin_bit_cast(float, __builtin_amdgcn_mov_dpp(__builtin_bit_cast(int, x), CTRL, 0xf, 0xf, true));
}
constexpr int XOR1 = 0xB1, XOR2 = 0x4E, XOR7 = 0x141, XOR8 = 0x128;

__device__ __forceinline__ float wave_sum(float v) {
#pragma unroll
  for (int o = 32; o > 0; o >>= 1) v += __shfl_xor(v, o, 64);
  return v;
}

__device__ __forceinline__ void layernorm_wave(const unsigned short* __restrict__ xr, int lane, float (&v)[7]) {
  float s = 0.f;
#pragma unroll
  for (int i = 0; i < 7; ++i) s += v[i];
  s = wave_sum(s);
  const float mean = s * (1.0f / DM);
  float ss = 0.f;
#pragma unroll
  for (int i = 0; i < 7; ++i) { const float d = v[i] - mean; ss += d * d; }
  ss = wave_sum(ss);
  const float var = ss * (1.0f / DM);
  const float mb = bfr(mean), vb = bfr(var);
  const float den = bfr(sqrtf(bfr(vb + 1e-5f)));
#pragma unroll
  for (int i = 0; i < 7; ++i) v[i] = bfr(bfr(v[i] - mb) / den);
}

__device__ __forceinline__ float gelu_tanh(float g) {
  const float u = 0.7978845608028654f * (g + 0.044715f * g * g * g);
  return 0.5f * g * (1.0f + tanhf(u));
}

using rsrc_t = __amdgpu_buffer_rsrc_t;
constexpr int KV_AUX = 2;
__device__ __forceinline__ u32x4 ldkv(rsrc_t r, unsigned voff, unsigned soff) {
  return __builtin_bit_cast(u32x4, __builtin_amdgcn_raw_buffer_load_b128(r, voff, soff, KV_AUX));
}

#define PIN16(a) asm volatile("" : "+v"(a[0]), "+v"(a[1]), "+v"(a[2]), "+v"(a[3]), "+v"(a[4]), "+v"(a[5]), "+v"(a[6]), "+v"(a[7]), \
    "+v"(a[8]), "+v"(a[9]), "+v"(a[10]), "+v"(a[11]), "+v"(a[12]), "+v"(a[13]), "+v"(a[14]), "+v"(a[15]) :: "memory")

template <bool PF>
__device__ __forceinline__ void chunk_body(u32x4 (&kb)[16], u32x4 (&vb)[16], rsrc_t kr, rsrc_t vr, unsigned voff, unsigned soff,
                                           const u32x4* qw, float (&acc)[16], float& m, float& l,
                                           float* pw, int lane) {
  constexpr float SC = 0.0625f * 1.4426950408889634f;
  const u32x4 q0 = qw[0], q1 = qw[32];
  float x[16];
#pragma unroll
  for (int i = 0; i < 16; ++i) {
    float s0 = 0.f, s1 = 0.f;
#pragma unroll
    for (int k = 0; k < 4; ++k) { s0 = dot2(kb[i][k], q0[k], s0); s1 = dot2(kb[i][k], q1[k], s1); }
    auto r = __builtin_amdgcn_permlane16_swap(__float_as_uint(s0), __float_as_uint(s1), false, false);
    x[i] = __uint_as_float(r[0]) + __uint_as_float(r[1]);
  }
#define TR_STEP(HALF, CTRL, BIT)                                                   \
  _Pragma("unroll") for (int h = 0; h < (HALF); ++h) {                             \
    const bool up = (lane & (BIT)) != 0;                                           \
    const float keep = up ? x[h + (HALF)] : x[h], send = up ? x[h] : x[h + (HALF)]; \
    x[h] = keep + dpp<CTRL>(send);                                                 \
  }
  TR_STEP(8, XOR8, 8) TR_STEP(4, XOR7, 4) TR_STEP(2, XOR2, 2) TR_STEP(1, XOR1, 1)
#undef TR_STEP
  asm volatile("" : "+v"(x[0]) :: "memory");
  if (PF) {
#pragma unroll
    for (int i = 0; i < 16; ++i) kb[i] = ldkv(kr, voff + (i & 3) * 1024, soff + (i >> 2) * 4096);
  }
  asm volatile("" ::: "memory");
  const float s = bfr(x[0]) * SC;
  float pm = s;
  pm = fmaxf(pm, dpp<XOR1>(pm)); pm = fmaxf(pm, dpp<XOR2>(pm)); pm = fmaxf(pm, dpp<XOR7>(pm)); pm = fmaxf(pm, dpp<XOR8>(pm));
  {
    auto r = __builtin_amdgcn_permlane32_swap(__float_as_uint(pm), __float_as_uint(pm), false, false);
    pm = fmaxf(__uint_as_float(r[0]), __uint_as_float(r[1]));
  }
  const float mn = fmaxf(m, pm);
  const float alpha = __builtin_amdgcn_exp2f(m - mn);
  const float p = __builtin_amdgcn_exp2f(s - mn);
  l = fmaf(l, alpha, p);
  m = mn;
  const float a0 = __uint_as_float(__builtin_amdgcn_readlane(__float_as_uint(alpha), 0));
  const float a1 = __uint_as_float(__builtin_amdgcn_readlane(__float_as_uint(alpha), 16));
#pragma unroll
  for (int k = 0; k < 8; ++k) { acc[k] *= a0; acc[8 + k] *= a1; }
  pw[lane] = p;
  asm volatile("s_waitcnt lgkmcnt(0)" ::: "memory");
  const float* prd = pw + (lane >> 5) * 32;
#pragma unroll
  for (int u = 0; u < 4; ++u) {
    const f32x4 pa = *reinterpret_cast<const f32x4*>(prd + 4 * u);
    const f32x4 pb = *reinterpret_cast<const f32x4*>(prd + 16 + 4 * u);
    asm volatile("" : "+v"(vb[4 * u]), "+v"(vb[4 * u + 1]), "+v"(vb[4 * u + 2]), "+v"(vb[4 * u + 3]));
#pragma unroll
    for (int j = 0; j < 4; ++j) {
      const int i = 4 * u + j;
      const float p0 = pa[j], p1 = pb[j];
#pragma unroll
      for (int k = 0; k < 4; ++k) {
        const float lo = bflo(vb[i][k]), hi = bfhi(vb[i][k]);
        acc[2 * k] = fmaf(p0, lo, acc[2 * k]); acc[2 * k + 1] = fmaf(p0, hi, acc[2 * k + 1]);
        acc[8 + 2 * k] = fmaf(p1, lo, acc[8 + 2 * k]); acc[8 + 2 * k + 1] = fmaf(p1, hi, acc[8 + 2 * k + 1]);
      }
    }
  }
  PIN16(acc);
  if (PF) {
#pragma unroll
    for (int i = 0; i < 16; ++i) vb[i] = ldkv(vr, voff + (i & 3) * 1024, soff + (i >> 2) * 4096);
  }
  asm volatile("" ::: "memory");
}

constexpr int HELP_WAVES = 4;
constexpr int ATT_THREADS = (ATT_WAVES + HELP_WAVES) * 64;
constexpr int FFN_WGS = FF / 8 / HELP_WAVES;
constexpr int L_P = 0, L_MRG = L_P + ATT_WAVES * 64, L_MLS = L_MRG + ATT_WAVES * 2 * DH, L_QP = L_MLS + ATT_WAVES * 4,
              L_XN = L_QP + HELP_WAVES * 512, L_QW = L_XN + HELP_WAVES * 2 * DM, L_END = L_QW + ATT_WAVES * 256;

__device__ __forceinline__ void helper_wave(const unsigned short* __restrict__ x, const unsigned short* __restrict__ wq,
                                            const unsigned short* __restrict__ wg, const unsigned short* __restrict__ wu,
                                            unsigned char* __restrict__ ws, float* lds, int hw, int lane, int b, int kvh,
                                            int rot) {
  float v0[7], v1[7];
#pragma unroll
  for (int i = 0; i < 7; ++i) { v0[i] = bf2f(x[lane + 64 * i]); v1[i] = bf2f(x[DM + lane + 64 * i]); }
  const u32x4* wp = reinterpret_cast<const u32x4*>(wq + kvh * 512) + lane;
  u32x4 wa[16], wb[16];
  {
    const u32x4* w0 = wp + (size_t)(64 * rot + 16 * hw) * 512;
#pragma unroll
    for (int j = 0; j < 16; ++j) wa[j] = w0[(size_t)j * 512];
  }
  layernorm_wave(x, lane, v0);
  layernorm_wave(x + DM, lane, v1);
  float* xw2 = lds + L_XN + hw * 2 * DM;
#pragma unroll
  for (int i = 0; i < 7; ++i) { xw2[lane + 64 * i] = v0[i]; xw2[DM + lane + 64 * i] = v1[i]; }
  asm volatile("s_waitcnt lgkmcnt(0)" ::: "memory");
  const float* xw = xw2 + b * DM;
  float acc[8];
#pragma unroll
  for (int k = 0; k < 8; ++k) acc[k] = 0.f;
#pragma unroll
  for (int n = 0; n < 7; ++n) {
    int ic = rot + n; ic = ic >= 7 ? ic - 7 : ic;
    int in_ = ic + 1; in_ = in_ >= 7 ? in_ - 7 : in_;
    if (n < 6) {
      const u32x4* w1 = wp + (size_t)(64 * in_ + 16 * hw) * 512;
#pragma unroll
      for (int j = 0; j < 16; ++j) { if (n & 1) wa[j] = w1[(size_t)j * 512]; else wb[j] = w1[(size_t)j * 512]; }
    }
    asm volatile("" ::: "memory");
    const float* xr = xw + 64 * ic + 16 * hw;
#pragma unroll
    for (int j4 = 0; j4 < 4; ++j4) {
      const f32x4 xs = *reinterpret_cast<const f32x4*>(xr + 4 * j4);
#pragma unroll
      for (int jj = 0; jj < 4; ++jj) {
        const int j = 4 * j4 + jj;
        if (n & 1) asm volatile("" : "+v"(wb[j])); else asm volatile("" : "+v"(wa[j]));
        const u32x4 w = (n & 1) ? wb[j] : wa[j];
#pragma unroll
        for (int k = 0; k < 4; ++k) {
          acc[2 * k] = fmaf(xs[jj], bflo(w[k]), acc[2 * k]);
          acc[2 * k + 1] = fmaf(xs[jj], bfhi(w[k]), acc[2 * k + 1]);
        }
        asm volatile("" : "+v"(acc[0]), "+v"(acc[1]), "+v"(acc[2]), "+v"(acc[3]), "+v"(acc[4]), "+v"(acc[5]), "+v"(acc[6]), "+v"(acc[7]));
      }
    }
  }
  float* qp = lds + L_QP + hw * 512 + lane * 8;
  *reinterpret_cast<f32x4*>(qp) = f32x4{acc[0], acc[1], acc[2], acc[3]};
  *reinterpret_cast<f32x4*>(qp + 4) = f32x4{acc[4], acc[5], acc[6], acc[7]};
  asm volatile("s_waitcnt lgkmcnt(0)" ::: "memory");
  __builtin_amdgcn_s_barrier();
  asm volatile("" ::: "memory");
  if (blockIdx.x < FFN_WGS) {
    const int task = blockIdx.x * HELP_WAVES + hw;
    u32x4 g[7], u[7];
#pragma unroll
    for (int i = 0; i < 7; ++i) {
      g[i] = *reinterpret_cast<const u32x4*>(wg + (size_t)(lane + 64 * i) * FF + 8 * task);
      u[i] = *reinterpret_cast<const u32x4*>(wu + (size_t)(lane + 64 * i) * FF + 8 * task);
    }
#pragma unroll
    for (int i = 0; i < 7; ++i) { v0[i] = xw2[lane + 64 * i]; v1[i] = xw2[DM + lane + 64 * i]; }
    float a[4][8];
#pragma unroll
    for (int m_ = 0; m_ < 4; ++m_)
#pragma unroll
      for (int k = 0; k < 8; ++k) a[m_][k] = 0.f;
#pragma unroll
    for (int i = 0; i < 7; ++i) {
      asm volatile("" : "+v"(g[i]), "+v"(u[i]));
#pragma unroll
      for (int k = 0; k < 4; ++k) {
        const float gl = bflo(g[i][k]), gh = bfhi(g[i][k]), ul = bflo(u[i][k]), uh = bfhi(u[i][k]);
        a[0][2 * k] = fmaf(v0[i], gl, a[0][2 * k]); a[0][2 * k + 1] = fmaf(v0[i], gh, a[0][2 * k + 1]);
        a[1][2 * k] = fmaf(v1[i], gl, a[1][2 * k]); a[1][2 * k + 1] = fmaf(v1[i], gh, a[1][2 * k + 1]);
        a[2][2 * k] = fmaf(v0[i], ul, a[2][2 * k]); a[2][2 * k + 1] = fmaf(v0[i], uh, a[2][2 * k + 1]);
        a[3][2 * k] = fmaf(v1[i], ul, a[3][2 * k]); a[3][2 * k + 1] = fmaf(v1[i], uh, a[3][2 * k + 1]);
      }
    }
#pragma unroll
    for (int m_ = 0; m_ < 4; ++m_)
#pragma unroll
      for (int k = 0; k < 8; ++k) a[m_][k] = wave_sum(a[m_][k]);
    if (lane == 0) {
#pragma unroll
      for (int bb = 0; bb < 2; ++bb) {
        u32x4 o;
#pragma unroll
        for (int k = 0; k < 4; ++k) {
          const unsigned lo = f2bf(bfr(gelu_tanh(bfr(a[bb][2 * k]))) * bfr(a[2 + bb][2 * k]));
          const unsigned hi = f2bf(bfr(gelu_tanh(bfr(a[bb][2 * k + 1]))) * bfr(a[2 + bb][2 * k + 1]));
          o[k] = lo | (hi << 16);
        }
        *reinterpret_cast<u32x4*>(ws + WS_H + (size_t)(bb * FF + 8 * task) * 2) = o;
      }
    }
  }
}

__global__ __launch_bounds__(ATT_THREADS, 3) void attn_kernel(const u32x4* __restrict__ K, const u32x4* __restrict__ V,
                                                              const unsigned short* __restrict__ x,
                                                              const unsigned short* __restrict__ wq,
                                                              const unsigned short* __restrict__ wg,
                                                              const unsigned short* __restrict__ wu,
                                                              unsigned char* __restrict__ ws) {
  __shared__ __attribute__((aligned(16))) float lds[L_END];
  float* plds = lds + L_P;
  float* mrg = lds + L_MRG;
  float* mls = lds + L_MLS;
  const int t = threadIdx.x, lane = t & 63, wave = __builtin_amdgcn_readfirstlane(t >> 6);
  const int kvh = blockIdx.x & 7, rest = blockIdx.x >> 3, b = rest & 1, split = rest >> 1;
  if (wave >= ATT_WAVES) {
    if (blockIdx.x == 255 && t < ATT_WAVES * 64 + 8) reinterpret_cast<unsigned*>(ws + WS_CNT)[(t & 7) * 16] = 0u;
    helper_wave(x, wq, wg, wu, ws, lds, wave - ATT_WAVES, lane, b, kvh, rest % 7);
    return;
  }
  const int bh = b * HKV + kvh;
  const int c = lane & 31;
  const size_t base = ((size_t)bh * SK + (size_t)split * KEYS_WG + (size_t)wave * KEYS_WAVE) * (DH / 8);
  const rsrc_t kr = __builtin_amdgcn_make_buffer_rsrc((void*)(K + base), 0, KEYS_WAVE * DH * 2, 0x00020000);
  const rsrc_t vr = __builtin_amdgcn_make_buffer_rsrc((void*)(V + base), 0, KEYS_WAVE * DH * 2, 0x00020000);
  const unsigned voff = lane * 16;
  u32x4 kb[16], vb[16];
#pragma unroll
  for (int i = 0; i < 16; ++i) kb[i] = ldkv(kr, voff + (i & 3) * 1024, (i >> 2) * 4096);
  asm volatile("" ::: "memory");
#pragma unroll
  for (int i = 0; i < 16; ++i) vb[i] = ldkv(vr, voff + (i & 3) * 1024, (i >> 2) * 4096);
  asm volatile("" ::: "memory");
  __builtin_amdgcn_s_barrier();
  asm volatile("" ::: "memory");
  u32x4* qw = reinterpret_cast<u32x4*>(lds + L_QW + wave * 256) + c;
  {
    const float* qp = lds + L_QP;
#pragma unroll
    for (int h = 0; h < 2; ++h) {
      float qs[8];
#pragma unroll
      for (int k = 0; k < 8; ++k) qs[k] = 0.f;
#pragma unroll
      for (int w = 0; w < HELP_WAVES; ++w) {
        const f32x4 a0 = *reinterpret_cast<const f32x4*>(qp + w * 512 + h * 256 + 8 * c);
        const f32x4 a1 = *reinterpret_cast<const f32x4*>(qp + w * 512 + h * 256 + 8 * c + 4);
#pragma unroll
        for (int k = 0; k < 4; ++k) { qs[k] += a0[k]; qs[4 + k] += a1[k]; }
      }
      u32x4 qq;
#pragma unroll
      for (int k = 0; k < 4; ++k) qq[k] = (unsigned)f2bf(qs[2 * k]) | ((unsigned)f2bf(qs[2 * k + 1]) << 16);
      qw[32 * h] = qq;
    }
  }
  asm volatile("s_waitcnt lgkmcnt(0)" ::: "memory");
  float acc[16];
#pragma unroll
  for (int k = 0; k < 16; ++k) acc[k] = 0.f;
  float m = -INFINITY, l = 0.f;
  float* pw = plds + wave * 64;
  for (int ch = 0; ch < NCHUNK - 1; ++ch)
    chunk_body<true>(kb, vb, kr, vr, voff, (ch + 1) * (CHUNK * DH * 2), qw, acc, m, l, pw, lane);
  chunk_body<false>(kb, vb, kr, vr, voff, 0u, qw, acc, m, l, pw, lane);
#pragma unroll
  for (int k = 0; k < 16; ++k) {
    auto r = __builtin_amdgcn_permlane32_swap(__float_as_uint(acc[k]), __float_as_uint(acc[k]), false, false);
    acc[k] = __uint_as_float(r[0]) + __uint_as_float(r[1]);
  }
  l += dpp<XOR1>(l); l += dpp<XOR2>(l); l += dpp<XOR7>(l); l += dpp<XOR8>(l);
  {
    auto r = __builtin_amdgcn_permlane32_swap(__float_as_uint(l), __float_as_uint(l), false, false);
    l = __uint_as_float(r[0]) + __uint_as_float(r[1]);
  }
  if (lane < 32) {
#pragma unroll
    for (int h = 0; h < 2; ++h) {
      *reinterpret_cast<f32x4*>(mrg + (wave * 2 + h) * DH + 8 * c) = f32x4{acc[8 * h + 0], acc[8 * h + 1], acc[8 * h + 2], acc[8 * h + 3]};
      *reinterpret_cast<f32x4*>(mrg + (wave * 2 + h) * DH + 8 * c + 4) = f32x4{acc[8 * h + 4], acc[8 * h + 5], acc[8 * h + 6], acc[8 * h + 7]};
    }
    if ((lane & 15) == 0) { const int h = lane >> 4; mls[(wave * 2 + h) * 2] = m; mls[(wave * 2 + h) * 2 + 1] = l; }
  }
  __syncthreads();
  {
    const int h = t >> 8, d = t & 255;
    float M = -INFINITY;
#pragma unroll
    for (int w = 0; w < ATT_WAVES; ++w) M = fmaxf(M, mls[(w * 2 + h) * 2]);
    float L = 0.f, o = 0.f;
#pragma unroll
    for (int w = 0; w < ATT_WAVES; ++w) {
      const float wt = __builtin_amdgcn_exp2f(mls[(w * 2 + h) * 2] - M);
      L = fmaf(wt, mls[(w * 2 + h) * 2 + 1], L);
      o = fmaf(wt, mrg[(w * 2 + h) * DH + d], o);
    }
    const int row = (b * HQ + kvh * 2 + h) * NSPLIT + split;
    reinterpret_cast<float*>(ws + WS_PO)[(size_t)row * DH + d] = o;
    if (d == 0) { reinterpret_cast<float*>(ws + WS_PM)[row] = M; reinterpret_cast<float*>(ws + WS_PL)[row] = L; }
  }
}

constexpr int KP_O = HQ * DH / 128;
constexpr int KP_D = FF / 128;
constexpr int KP = KP_O + KP_D;
constexpr int CG = DM / 64;

__global__ __launch_bounds__(256) void tail_kernel(const unsigned short* __restrict__ x,
                                                   const unsigned short* __restrict__ wo,
                                                   const unsigned short* __restrict__ wd,
                                                   unsigned char* __restrict__ ws, unsigned short* __restrict__ out) {
  __shared__ float lds[2 * 128 + 4 * 2 * 64 + 4];
  float* vec = lds;
  float* red = lds + 256;
  unsigned* flag = reinterpret_cast<unsigned*>(lds + 256 + 512);
  const int t = threadIdx.x, lane = t & 63, wave = t >> 6;
  const int cg = blockIdx.x % CG, kp = blockIdx.x / CG;
  const int cq = t & 7, rg = t >> 3;
  const unsigned short* W = kp < KP_O ? wo + (size_t)kp * 128 * DM : wd + (size_t)(kp - KP_O) * 128 * DM;
  u32x4 w[4];
#pragma unroll
  for (int i = 0; i < 4; ++i) w[i] = *reinterpret_cast<const u32x4*>(W + (size_t)(rg + 32 * i) * DM + cg * 64 + cq * 8);
  {
    const int b = t >> 7, d = t & 127;
    if (kp < KP_O) {
      const int head = kp >> 1, dh = kp & 1;
      const int row0 = (b * HQ + head) * NSPLIT;
      const float* pm = reinterpret_cast<const float*>(ws + WS_PM) + row0;
      const float* pl = reinterpret_cast<const float*>(ws + WS_PL) + row0;
      const float* po = reinterpret_cast<const float*>(ws + WS_PO) + (size_t)row0 * DH + dh * 128 + d;
      float ms[NSPLIT], ls[NSPLIT], os[NSPLIT];
#pragma unroll
      for (int s = 0; s < NSPLIT; ++s) { ms[s] = pm[s]; ls[s] = pl[s]; os[s] = po[(size_t)s * DH]; }
      float M = -INFINITY;
#pragma unroll
      for (int s = 0; s < NSPLIT; ++s) M = fmaxf(M, ms[s]);
      float L = 0.f, o = 0.f;
#pragma unroll
      for (int s = 0; s < NSPLIT; ++s) { const float wt = __builtin_amdgcn_exp2f(ms[s] - M); L = fmaf(wt, ls[s], L); o = fmaf(wt, os[s], o); }
      vec[b * 128 + d] = bfr(o / L);
    } else {
      vec[b * 128 + d] = bf2f(reinterpret_cast<const unsigned short*>(ws + WS_H)[b * FF + (kp - KP_O) * 128 + d]);
    }
  }
  __syncthreads();
  float acc[2][8];
#pragma unroll
  for (int b = 0; b < 2; ++b)
#pragma unroll
    for (int k = 0; k < 8; ++k) acc[b][k] = 0.f;
#pragma unroll
  for (int i = 0; i < 4; ++i) {
    const float x0 = vec[rg + 32 * i], x1 = vec[128 + rg + 32 * i];
#pragma unroll
    for (int k = 0; k < 4; ++k) {
      const float lo = bflo(w[i][k]), hi = bfhi(w[i][k]);
      acc[0][2 * k] = fmaf(x0, lo, acc[0][2 * k]); acc[0][2 * k + 1] = fmaf(x0, hi, acc[0][2 * k + 1]);
      acc[1][2 * k] = fmaf(x1, lo, acc[1][2 * k]); acc[1][2 * k + 1] = fmaf(x1, hi, acc[1][2 * k + 1]);
    }
  }
#pragma unroll
  for (int b = 0; b < 2; ++b)
#pragma unroll
    for (int k = 0; k < 8; ++k) {
      float v = acc[b][k];
      v += __shfl_xor(v, 8, 64); v += __shfl_xor(v, 16, 64); v += __shfl_xor(v, 32, 64);
      acc[b][k] = v;
    }
  if (lane < 8) {
#pragma unroll
    for (int b = 0; b < 2; ++b)
#pragma unroll
      for (int k = 0; k < 8; ++k) red[(wave * 2 + b) * 64 + lane * 8 + k] = acc[b][k];
  }
  __syncthreads();
  float* part = reinterpret_cast<float*>(ws + WS_PART) + (size_t)cg * KP * 128;
  if (t < 128) {
    const int b = t >> 6, cc = t & 63;
    const float v = red[(0 * 2 + b) * 64 + cc] + red[(1 * 2 + b) * 64 + cc] + red[(2 * 2 + b) * 64 + cc] + red[(3 * 2 + b) * 64 + cc];
    __hip_atomic_store(part + kp * 128 + t, v, __ATOMIC_RELAXED, __HIP_MEMORY_SCOPE_AGENT);
  }
  asm volatile("s_waitcnt vmcnt(0)" ::: "memory");
  __syncthreads();
  if (t == 0) {
    unsigned* cnt = reinterpret_cast<unsigned*>(ws + WS_CNT) + cg * 16;
    const unsigned old = __hip_atomic_fetch_add(cnt, 1u, __ATOMIC_RELAXED, __HIP_MEMORY_SCOPE_AGENT);
    const unsigned last = (old == (unsigned)(KP - 1)) ? 1u : 0u;
    if (last) { __builtin_amdgcn_fence(__ATOMIC_ACQUIRE, "agent"); asm volatile("s_waitcnt vmcnt(0)" ::: "memory"); }
    flag[0] = last;
  }
  __syncthreads();
  if (flag[0] == 0u) return;
  if (t < 128) {
    const int b = t >> 6, cc = t & 63;
    float v[KP];
#pragma unroll
    for (int k = 0; k < KP; ++k) v[k] = __hip_atomic_load(part + k * 128 + t, __ATOMIC_RELAXED, __HIP_MEMORY_SCOPE_AGENT);
    float A = 0.f, Bf = 0.f;
#pragma unroll
    for (int k = 0; k < KP_O; ++k) A += v[k];
#pragma unroll
    for (int k = KP_O; k < KP; ++k) Bf += v[k];
    const float xv = bf2f(x[b * DM + cg * 64 + cc]);
    out[b * DM + cg * 64 + cc] = f2bf(bfr(xv + bfr(A)) + bfr(Bf));
  }
}

extern "C" void kernel_launch(void* const* d_in, const int* in_sizes, int n_in, void* d_out, int out_size, void* d_ws,
                              size_t ws_size, hipStream_t stream) {
  const unsigned short* x = (const unsigned short*)d_in[0];
  const u32x4* K = (const u32x4*)d_in[1];
  const u32x4* V = (const u32x4*)d_in[2];
  const unsigned short* wq = (const unsigned short*)d_in[3];
  const unsigned short* wo = (const unsigned short*)d_in[4];
  const unsigned short* wg = (const unsigned short*)d_in[6];
  const unsigned short* wu = (const unsigned short*)d_in[7];
  const unsigned short* wd = (const unsigned short*)d_in[8];
  unsigned char* ws = (unsigned char*)d_ws;
  attn_kernel<<<NB * HKV * NSPLIT, ATT_THREADS, 0, stream>>>(K, V, x, wq, wg, wu, ws);
  tail_kernel<<<KP * CG, 256, 0, stream>>>(x, wo, wd, ws, (unsigned short*)d_out);
}
```

```cpp
#include <hip/hip_runtime.h>
#include <hip/hip_bf16.h>
#include <stdint.h>

constexpr int NB = 2, DM = 448, HQ = 16, HKV = 8, SK = 32768, DH = 256, FF = 1792;
constexpr int NSPLIT = 16;
#define AW 4
constexpr int ATT_WAVES = AW;
constexpr int CHUNK = 32;
constexpr int NCHUNK_BH = SK / CHUNK;
constexpr int WAVES_BH = NSPLIT * ATT_WAVES;
constexpr int CHUNK_BYTES = CHUNK * DH * 2;

constexpr size_t WS_CNT = 0;
constexpr int KPART = 8;
constexpr size_t WS_Q = 4096;
constexpr size_t WS_H = WS_Q + (size_t)KPART * NB * HQ * DH * 4;
constexpr size_t WS_PM = WS_H + (size_t)2 * KPART * NB * FF * 4;
constexpr size_t WS_PL = WS_PM + NB * HQ * NSPLIT * 4;
constexpr size_t WS_PO = WS_PL + NB * HQ * NSPLIT * 4;
constexpr size_t WS_PART = WS_PO + (size_t)NB * HQ * NSPLIT * DH * 4;

using u32x4 = __attribute__((ext_vector_type(4))) unsigned;
using f32x4 = __attribute__((ext_vector_type(4))) float;
using bf16x2 = __attribute__((ext_vector_type(2))) __bf16;
using rsrc_t = __amdgpu_buffer_rsrc_t;

__device__ __forceinline__ float bf2f(unsigned short u) { return __uint_as_float(((unsigned)u) << 16); }
__device__ __forceinline__ float bflo(unsigned u) { return __uint_as_float(u << 16); }
__device__ __forceinline__ float bfhi(unsigned u) { return __uint_as_float(u & 0xffff0000u); }
__device__ __forceinline__ unsigned short f2bf(float x) { __bf16 h = (__bf16)x; return __builtin_bit_cast(unsigned short, h); }
__device__ __forceinline__ float bfr(float x) { __bf16 h = (__bf16)x; return (float)h; }
__device__ __forceinline__ float dot2(unsigned a, unsigned b, float acc) {
  return __builtin_amdgcn_fdot2_f32_bf16(__builtin_bit_cast(bf16x2, a), __builtin_bit_cast(bf16x2, b), acc, false);
}
template <int CTRL> __device__ __forceinline__ float dpp(float x) {
  return __builtin_bit_cast(float, __builtin_amdgcn_mov_dpp(__builtin_bit_cast(int, x), CTRL, 0xf, 0xf, true));
}
constexpr int XOR1 = 0xB1, XOR2 = 0x4E, XOR7 = 0x141, XOR8 = 0x128;

__device__ __forceinline__ float wave_sum(float v) {
#pragma unroll
  for (int o = 32; o > 0; o >>= 1) v += __shfl_xor(v, o, 64);
  return v;
}

__device__ __forceinline__ void layernorm_wave(float (&v)[7]) {
  float s = 0.f;
#pragma unroll
  for (int i = 0; i < 7; ++i) s += v[i];
  s = wave_sum(s);
  const float mean = s * (1.0f / DM);
  float ss = 0.f;
#pragma unroll
  for (int i = 0; i < 7; ++i) { const float d = v[i] - mean; ss += d * d; }
  ss = wave_sum(ss);
  const float var = ss * (1.0f / DM);
  const float mb = bfr(mean), vb = bfr(var);
  const float den = bfr(sqrtf(bfr(vb + 1e-5f)));
#pragma unroll
  for (int i = 0; i < 7; ++i) v[i] = bfr(bfr(v[i] - mb) / den);
}

__device__ __forceinline__ float gelu_tanh(float g) {
  const float u = 0.7978845608028654f * (g + 0.044715f * g * g * g);
  return 0.5f * g * (1.0f + tanhf(u));
}

constexpr int PREP_Q_WGS = (HQ * DH / 256) * KPART;
constexpr int PREP_F_WGS = (FF / 256) * KPART * 2;

__global__ __launch_bounds__(256) void prep_kernel(const unsigned short* __restrict__ x,
                                                   const unsigned short* __restrict__ wq,
                                                   const unsigned short* __restrict__ wg,
                                                   const unsigned short* __restrict__ wu,
                                                   unsigned char* __restrict__ ws) {
  __shared__ __attribute__((aligned(16))) float lds[2 * DM + 4 * 2 * 256];
  float* xn = lds;
  float* red = lds + 2 * DM;
  const int t = threadIdx.x, wave = t >> 6, lane = t & 63;
  const int cl = t & 31, rg = t >> 5;
  const unsigned short* W;
  int ldw, kp;
  float* dst;
  if (blockIdx.x < PREP_Q_WGS) {
    const int cgp = blockIdx.x & 15; kp = blockIdx.x >> 4;
    W = wq + cgp * 256; ldw = HQ * DH;
    dst = reinterpret_cast<float*>(ws + WS_Q) + (size_t)kp * NB * HQ * DH + cgp * 256;
  } else {
    const int j = blockIdx.x - PREP_Q_WGS, mat = j / 56, r = j % 56, cgp = r % 7; kp = r / 7;
    W = (mat ? wu : wg) + cgp * 256; ldw = FF;
    dst = reinterpret_cast<float*>(ws + WS_H) + (size_t)(mat * KPART + kp) * NB * FF + cgp * 256;
  }
  const int row0 = kp * 56 + rg;
  float v[7];
  if (t < 128) {
#pragma unroll
    for (int i = 0; i < 7; ++i) v[i] = bf2f(x[wave * DM + lane + 64 * i]);
  }
  u32x4 w[7];
#pragma unroll
  for (int i = 0; i < 7; ++i) w[i] = *reinterpret_cast<const u32x4*>(W + (size_t)(row0 + 8 * i) * ldw + cl * 8);
  if (blockIdx.x == 0 && t < 16) reinterpret_cast<unsigned*>(ws + WS_CNT)[t * 64] = 0u;
  if (t < 128) {
    layernorm_wave(v);
#pragma unroll
    for (int i = 0; i < 7; ++i) xn[wave * DM + lane + 64 * i] = v[i];
  }
  __syncthreads();
  float acc[2][8];
#pragma unroll
  for (int b = 0; b < 2; ++b)
#pragma unroll
    for (int k = 0; k < 8; ++k) acc[b][k] = 0.f;
#pragma unroll
  for (int i = 0; i < 7; ++i) {
    const float x0 = xn[row0 + 8 * i], x1 = xn[DM + row0 + 8 * i];
#pragma unroll
    for (int k = 0; k < 4; ++k) {
      const float lo = bflo(w[i][k]), hi = bfhi(w[i][k]);
      acc[0][2 * k] = fmaf(x0, lo, acc[0][2 * k]); acc[0][2 * k + 1] = fmaf(x0, hi, acc[0][2 * k + 1]);
      acc[1][2 * k] = fmaf(x1, lo, acc[1][2 * k]); acc[1][2 * k + 1] = fmaf(x1, hi, acc[1][2 * k + 1]);
    }
  }
#pragma unroll
  for (int b = 0; b < 2; ++b)
#pragma unroll
    for (int k = 0; k < 8; ++k) acc[b][k] += __shfl_xor(acc[b][k], 32, 64);
  if (lane < 32) {
#pragma unroll
    for (int b = 0; b < 2; ++b) {
      float* r = red + (wave * 2 + b) * 256 + lane * 8;
      *reinterpret_cast<f32x4*>(r) = f32x4{acc[b][0], acc[b][1], acc[b][2], acc[b][3]};
      *reinterpret_cast<f32x4*>(r + 4) = f32x4{acc[b][4], acc[b][5], acc[b][6], acc[b][7]};
    }
  }
  __syncthreads();
#pragma unroll
  for (int b = 0; b < 2; ++b) {
    const float* r = red + b * 256 + t;
    dst[(size_t)b * ldw + t] = (r[0] + r[512]) + (r[1024] + r[1536]);
  }
}

constexpr int KV_AUX = 2;
__device__ __forceinline__ u32x4 ldkv(rsrc_t r, unsigned voff, unsigned soff) {
  return __builtin_bit_cast(u32x4, __builtin_amdgcn_raw_buffer_load_b128(r, voff, soff, KV_AUX));
}

#define PERM 0
#define CHMAP(c) (c)
#define PIN16(a) asm volatile("" : "+v"(a[0]), "+v"(a[1]), "+v"(a[2]), "+v"(a[3]), "+v"(a[4]), "+v"(a[5]), "+v"(a[6]), "+v"(a[7]), \
    "+v"(a[8]), "+v"(a[9]), "+v"(a[10]), "+v"(a[11]), "+v"(a[12]), "+v"(a[13]), "+v"(a[14]), "+v"(a[15]) :: "memory")

__global__ __launch_bounds__(ATT_WAVES * 64, ATT_WAVES / 4) void attn_kernel(const unsigned short* __restrict__ Kc, const unsigned short* __restrict__ Vc,
                                                      unsigned char* __restrict__ ws) {
  __shared__ __attribute__((aligned(16))) float lds[ATT_WAVES * 64 + ATT_WAVES * 2 * DH + ATT_WAVES * 4];
  float* plds = lds;
  float* mrg = lds + ATT_WAVES * 64;
  float* mls = mrg + ATT_WAVES * 2 * DH;
  const int t = threadIdx.x, lane = t & 63, wave = __builtin_amdgcn_readfirstlane(t >> 6);
#define MAP 1
#define DYN 1
  const int bh = blockIdx.x >> 4, split = blockIdx.x & 15;
  const int b = bh >> 3, kvh = bh & 7;
  {
    constexpr int NG = 7 * 46 * 128;
    for (int gi = blockIdx.x * (ATT_WAVES * 64) + t; gi < NG; gi += 256 * ATT_WAVES * 64)
      reinterpret_cast<unsigned long long*>(ws + WS_PART)[gi] = 0ull;
  }
  const int c = lane & 31;
  const size_t base = (size_t)bh * SK * DH;
  const rsrc_t kr = __builtin_amdgcn_make_buffer_rsrc((void*)(Kc + base), 0, SK * DH * 2, 0x00020000);
  const rsrc_t vr = __builtin_amdgcn_make_buffer_rsrc((void*)(Vc + base), 0, SK * DH * 2, 0x00020000);
  unsigned* ctr = reinterpret_cast<unsigned*>(ws + WS_CNT) + bh * 64;
  u32x4 q0, q1;
  {
    const float* qg = reinterpret_cast<const float*>(ws + WS_Q) + (size_t)b * HQ * DH + kvh * 512 + 8 * c;
    f32x4 qa[2][2];
#pragma unroll
    for (int h = 0; h < 2; ++h) { qa[h][0] = f32x4{0.f, 0.f, 0.f, 0.f}; qa[h][1] = qa[h][0]; }
#pragma unroll
    for (int kp = 0; kp < KPART; ++kp)
#pragma unroll
      for (int h = 0; h < 2; ++h) {
        qa[h][0] += *reinterpret_cast<const f32x4*>(qg + (size_t)kp * NB * HQ * DH + h * 256);
        qa[h][1] += *reinterpret_cast<const f32x4*>(qg + (size_t)kp * NB * HQ * DH + h * 256 + 4);
      }
#pragma unroll
    for (int k = 0; k < 4; ++k) {
      q0[k] = (unsigned)f2bf(qa[0][k >> 1][(2 * k) & 3]) | ((unsigned)f2bf(qa[0][k >> 1][(2 * k + 1) & 3]) << 16);
      q1[k] = (unsigned)f2bf(qa[1][k >> 1][(2 * k) & 3]) | ((unsigned)f2bf(qa[1][k >> 1][(2 * k + 1) & 3]) << 16);
    }
  }
  const unsigned rank = split * ATT_WAVES + wave;
  unsigned cur = rank, nxt = WAVES_BH + rank;
  u32x4 kb[16], vb[16];
  asm volatile("" ::: "memory");
  {
    const unsigned vo = CHMAP(cur) * CHUNK_BYTES + lane * 16;
#pragma unroll
    for (int i = 0; i < 16; ++i) kb[i] = ldkv(kr, vo + (i & 3) * 1024, (i >> 2) * 4096);
    __builtin_amdgcn_sched_barrier(0);
#pragma unroll
    for (int i = 0; i < 16; ++i) vb[i] = ldkv(vr, vo + (i & 3) * 1024, (i >> 2) * 4096);
    __builtin_amdgcn_sched_barrier(0);
  }
  float acc[16];
#pragma unroll
  for (int k = 0; k < 16; ++k) acc[k] = 0.f;
  float m = -INFINITY, l = 0.f;
  float* pw = plds + wave * 64;
  constexpr float SC = 0.0625f * 1.4426950408889634f;
  do {
    float xx[16];
#pragma unroll
    for (int i = 0; i < 16; ++i) {
      float s0 = 0.f, s1 = 0.f;
#pragma unroll
      for (int k = 0; k < 4; ++k) { s0 = dot2(kb[i][k], q0[k], s0); s1 = dot2(kb[i][k], q1[k], s1); }
      auto r = __builtin_amdgcn_permlane16_swap(__float_as_uint(s0), __float_as_uint(s1), false, false);
      xx[i] = __uint_as_float(r[0]) + __uint_as_float(r[1]);
    }
    PIN16(xx);
    unsigned claim = 0;
    {
      unsigned long long sv;
      asm volatile("s_mov_b64 %1, exec\n\ts_mov_b64 exec, 1\n\tglobal_atomic_add %0, %2, %3, %4 sc0\n\ts_mov_b64 exec, %1"
                   : "=&v"(claim), "=&s"(sv) : "v"(0u), "v"(1u), "s"(ctr) : "memory");
    }
    const unsigned von = CHMAP(nxt) * CHUNK_BYTES + lane * 16;
#pragma unroll
    for (int i = 0; i < 16; ++i) kb[i] = ldkv(kr, von + (i & 3) * 1024, (i >> 2) * 4096);
    asm volatile("" ::: "memory");
#define TR_STEP(HALF, CTRL, BIT)                                                       \
  _Pragma("unroll") for (int h = 0; h < (HALF); ++h) {                                 \
    const bool up = (lane & (BIT)) != 0;                                               \
    const float keep = up ? xx[h + (HALF)] : xx[h], send = up ? xx[h] : xx[h + (HALF)]; \
    xx[h] = keep + dpp<CTRL>(send);                                                    \
  }
    TR_STEP(8, XOR8, 8) TR_STEP(4, XOR7, 4) TR_STEP(2, XOR2, 2) TR_STEP(1, XOR1, 1)
#undef TR_STEP
    const float s = bfr(xx[0]) * SC;
    float pm = s;
    pm = fmaxf(pm, dpp<XOR1>(pm)); pm = fmaxf(pm, dpp<XOR2>(pm)); pm = fmaxf(pm, dpp<XOR7>(pm)); pm = fmaxf(pm, dpp<XOR8>(pm));
    {
      auto r = __builtin_amdgcn_permlane32_swap(__float_as_uint(pm), __float_as_uint(pm), false, false);
      pm = fmaxf(__uint_as_float(r[0]), __uint_as_float(r[1]));
    }
    const float mn = fmaxf(m, pm);
    const float alpha = __builtin_amdgcn_exp2f(m - mn);
    const float p = __builtin_amdgcn_exp2f(s - mn);
    l = fmaf(l, alpha, p);
    m = mn;
    const float a0 = __uint_as_float(__builtin_amdgcn_readlane(__float_as_uint(alpha), 0));
    const float a1 = __uint_as_float(__builtin_amdgcn_readlane(__float_as_uint(alpha), 16));
#pragma unroll
    for (int k = 0; k < 8; ++k) { acc[k] *= a0; acc[8 + k] *= a1; }
    pw[lane] = p;
    asm volatile("s_waitcnt lgkmcnt(0)" ::: "memory");
    const float* prd = pw + (lane >> 5) * 32;
#pragma unroll
    for (int u = 0; u < 4; ++u) {
      const f32x4 pa = *reinterpret_cast<const f32x4*>(prd + 4 * u);
      const f32x4 pb = *reinterpret_cast<const f32x4*>(prd + 16 + 4 * u);
      asm volatile("" : "+v"(vb[4 * u]), "+v"(vb[4 * u + 1]), "+v"(vb[4 * u + 2]), "+v"(vb[4 * u + 3]));
#pragma unroll
      for (int j = 0; j < 4; ++j) {
        const int i = 4 * u + j;
        const float p0 = pa[j], p1 = pb[j];
#pragma unroll
        for (int k = 0; k < 4; ++k) {
          const float lo = bflo(vb[i][k]), hi = bfhi(vb[i][k]);
          acc[2 * k] = fmaf(p0, lo, acc[2 * k]); acc[2 * k + 1] = fmaf(p0, hi, acc[2 * k + 1]);
          acc[8 + 2 * k] = fmaf(p1, lo, acc[8 + 2 * k]); acc[8 + 2 * k + 1] = fmaf(p1, hi, acc[8 + 2 * k + 1]);
        }
      }
    }
    PIN16(acc);
#pragma unroll
    for (int i = 0; i < 16; ++i) vb[i] = ldkv(vr, von + (i & 3) * 1024, (i >> 2) * 4096);
    asm volatile("s_waitcnt vmcnt(32)" : "+v"(claim) :: "memory");
    cur = nxt;
    nxt = 2 * WAVES_BH + __builtin_amdgcn_readfirstlane(claim);
  } while (cur < NCHUNK_BH);
#pragma unroll
  for (int k = 0; k < 16; ++k) {
    auto r = __builtin_amdgcn_permlane32_swap(__float_as_uint(acc[k]), __float_as_uint(acc[k]), false, false);
    acc[k] = __uint_as_float(r[0]) + __uint_as_float(r[1]);
  }
  l += dpp<XOR1>(l); l += dpp<XOR2>(l); l += dpp<XOR7>(l); l += dpp<XOR8>(l);
  {
    auto r = __builtin_amdgcn_permlane32_swap(__float_as_uint(l), __float_as_uint(l), false, false);
    l = __uint_as_float(r[0]) + __uint_as_float(r[1]);
  }
  if (lane < 32) {
#pragma unroll
    for (int h = 0; h < 2; ++h) {
      *reinterpret_cast<f32x4*>(mrg + (wave * 2 + h) * DH + 8 * c) = f32x4{acc[8 * h + 0], acc[8 * h + 1], acc[8 * h + 2], acc[8 * h + 3]};
      *reinterpret_cast<f32x4*>(mrg + (wave * 2 + h) * DH + 8 * c + 4) = f32x4{acc[8 * h + 4], acc[8 * h + 5], acc[8 * h + 6], acc[8 * h + 7]};
    }
    if ((lane & 15) == 0) { const int h = lane >> 4; mls[(wave * 2 + h) * 2] = m; mls[(wave * 2 + h) * 2 + 1] = l; }
  }
  __syncthreads();
  for (int tt = t; tt < 512; tt += ATT_WAVES * 64) {
    const int h = tt >> 8, d = tt & 255;
    float M = -INFINITY;
#pragma unroll
    for (int w = 0; w < ATT_WAVES; ++w) M = fmaxf(M, mls[(w * 2 + h) * 2]);
    float L = 0.f, o = 0.f;
#pragma unroll
    for (int w = 0; w < ATT_WAVES; ++w) {
      const float wt = __builtin_amdgcn_exp2f(mls[(w * 2 + h) * 2] - M);
      L = fmaf(wt, mls[(w * 2 + h) * 2 + 1], L);
      o = fmaf(wt, mrg[(w * 2 + h) * DH + d], o);
    }
    const int row = (b * HQ + kvh * 2 + h) * NSPLIT + split;
    reinterpret_cast<float*>(ws + WS_PO)[(size_t)row * DH + d] = o;
    if (d == 0) { reinterpret_cast<float*>(ws + WS_PM)[row] = M; reinterpret_cast<float*>(ws + WS_PL)[row] = L; }
  }
}

constexpr int KP_O = HQ * DH / 128;
constexpr int KP_D = FF / 128;
constexpr int KP = KP_O + KP_D;
constexpr int CG = DM / 64;

__device__ __forceinline__ void tail_reduce(const unsigned short* __restrict__ x, unsigned char* __restrict__ ws,
                                            unsigned short* __restrict__ out, int cg, float* lds) {
  const int t = threadIdx.x, half = t >> 7, bc = t & 127;
  unsigned long long* part = reinterpret_cast<unsigned long long*>(ws + WS_PART) + (size_t)cg * KP * 128 + bc;
  float v[23];
  for (unsigned spin = 0; spin < (1u << 24); ++spin) {
    bool ok = true;
#pragma unroll
    for (int k = 0; k < 23; ++k) {
      const unsigned long long g = __hip_atomic_load(part + (half * 23 + k) * 128, __ATOMIC_RELAXED, __HIP_MEMORY_SCOPE_AGENT);
      v[k] = __uint_as_float((unsigned)g);
      ok &= (g >> 32) == 1ull;
    }
    if (__all(ok)) break;
    __builtin_amdgcn_s_sleep(1);
  }
  float A = 0.f, Bf = 0.f;
#pragma unroll
  for (int k = 0; k < 23; ++k) { if (half * 23 + k < KP_O) A += v[k]; else Bf += v[k]; }
  if (half == 1) { lds[bc] = A; lds[128 + bc] = Bf; }
  __syncthreads();
  if (half == 0) {
    A += lds[bc]; Bf = lds[128 + bc];
    const int b = bc >> 6, cc = bc & 63;
    const float xv = bf2f(x[b * DM + cg * 64 + cc]);
    out[b * DM + cg * 64 + cc] = f2bf(bfr(xv + bfr(A)) + bfr(Bf));
  }
}

__global__ __launch_bounds__(256) void tail_kernel(const unsigned short* __restrict__ x,
                                                   const unsigned short* __restrict__ wo,
                                                   const unsigned short* __restrict__ wd,
                                                   unsigned char* __restrict__ ws, unsigned short* __restrict__ out) {
  __shared__ float lds[2 * 128 + 4 * 2 * 64 + 4];
  float* vec = lds;
  float* red = lds + 256;
  const int t = threadIdx.x, lane = t & 63, wave = t >> 6;
  if (blockIdx.x >= KP * CG) {
    tail_reduce(x, ws, out, blockIdx.x - KP * CG, lds);
    return;
  }
  const int cg = blockIdx.x % CG, kp = blockIdx.x / CG;
  const int cq = t & 7, rg = t >> 3;
  const unsigned short* W = kp < KP_O ? wo + (size_t)kp * 128 * DM : wd + (size_t)(kp - KP_O) * 128 * DM;
  u32x4 w[4];
#pragma unroll
  for (int i = 0; i < 4; ++i) w[i] = *reinterpret_cast<const u32x4*>(W + (size_t)(rg + 32 * i) * DM + cg * 64 + cq * 8);
  {
    const int b = t >> 7, d = t & 127;
    if (kp < KP_O) {
      const int head = kp >> 1, dh = kp & 1;
      const int row0 = (b * HQ + head) * NSPLIT;
      const float* pm = reinterpret_cast<const float*>(ws + WS_PM) + row0;
      const float* pl = reinterpret_cast<const float*>(ws + WS_PL) + row0;
      const float* po = reinterpret_cast<const float*>(ws + WS_PO) + (size_t)row0 * DH + dh * 128 + d;
      float ms[NSPLIT], ls[NSPLIT], os[NSPLIT];
#pragma unroll
      for (int s = 0; s < NSPLIT; ++s) { ms[s] = pm[s]; ls[s] = pl[s]; os[s] = po[(size_t)s * DH]; }
      float M = -INFINITY;
#pragma unroll
      for (int s = 0; s < NSPLIT; ++s) M = fmaxf(M, ms[s]);
      float L = 0.f, o = 0.f;
#pragma unroll
      for (int s = 0; s < NSPLIT; ++s) { const float wt = __builtin_amdgcn_exp2f(ms[s] - M); L = fmaf(wt, ls[s], L); o = fmaf(wt, os[s], o); }
      vec[b * 128 + d] = bfr(o / L);
    } else {
      const float* gp = reinterpret_cast<const float*>(ws + WS_H) + (size_t)b * FF + (kp - KP_O) * 128 + d;
      float g = 0.f, u = 0.f;
#pragma unroll
      for (int k8 = 0; k8 < KPART; ++k8) { g += gp[(size_t)k8 * NB * FF]; u += gp[(size_t)(KPART + k8) * NB * FF]; }
      vec[b * 128 + d] = bfr(bfr(gelu_tanh(bfr(g))) * bfr(u));
    }
  }
  __syncthreads();
  float acc[2][8];
#pragma unroll
  for (int b = 0; b < 2; ++b)
#pragma unroll
    for (int k = 0; k < 8; ++k) acc[b][k] = 0.f;
#pragma unroll
  for (int i = 0; i < 4; ++i) {
    const float x0 = vec[rg + 32 * i], x1 = vec[128 + rg + 32 * i];
#pragma unroll
    for (int k = 0; k < 4; ++k) {
      const float lo = bflo(w[i][k]), hi = bfhi(w[i][k]);
      acc[0][2 * k] = fmaf(x0, lo, acc[0][2 * k]); acc[0][2 * k + 1] = fmaf(x0, hi, acc[0][2 * k + 1]);
      acc[1][2 * k] = fmaf(x1, lo, acc[1][2 * k]); acc[1][2 * k + 1] = fmaf(x1, hi, acc[1][2 * k + 1]);
    }
  }
#pragma unroll
  for (int b = 0; b < 2; ++b)
#pragma unroll
    for (int k = 0; k < 8; ++k) {
      float v = acc[b][k];
      v += __shfl_xor(v, 8, 64); v += __shfl_xor(v, 16, 64); v += __shfl_xor(v, 32, 64);
      acc[b][k] = v;
    }
  if (lane < 8) {
#pragma unroll
    for (int b = 0; b < 2; ++b)
#pragma unroll
      for (int k = 0; k < 8; ++k) red[(wave * 2 + b) * 64 + lane * 8 + k] = acc[b][k];
  }
  __syncthreads();
  unsigned long long* part = reinterpret_cast<unsigned long long*>(ws + WS_PART) + (size_t)cg * KP * 128;
  if (t < 128) {
    const int b = t >> 6, cc = t & 63;
    const float v = red[(0 * 2 + b) * 64 + cc] + red[(1 * 2 + b) * 64 + cc] + red[(2 * 2 + b) * 64 + cc] + red[(3 * 2 + b) * 64 + cc];
    __hip_atomic_store(part + kp * 128 + t, (1ull << 32) | (unsigned long long)__float_as_uint(v), __ATOMIC_RELAXED,
                       __HIP_MEMORY_SCOPE_AGENT);
  }
}

extern "C" void kernel_launch(void* const* d_in, const int* in_sizes, int n_in, void* d_out, int out_size, void* d_ws,
                              size_t ws_size, hipStream_t stream) {
  const unsigned short* x = (const unsigned short*)d_in[0];
  const unsigned short* K = (const unsigned short*)d_in[1];
  const unsigned short* V = (const unsigned short*)d_in[2];
  const unsigned short* wq = (const unsigned short*)d_in[3];
  const unsigned short* wo = (const unsigned short*)d_in[4];
  const unsigned short* wg = (const unsigned short*)d_in[6];
  const unsigned short* wu = (const unsigned short*)d_in[7];
  const unsigned short* wd = (const unsigned short*)d_in[8];
  unsigned char* ws = (unsigned char*)d_ws;
  prep_kernel<<<PREP_Q_WGS + PREP_F_WGS, 256, 0, stream>>>(x, wq, wg, wu, ws);
  attn_kernel<<<NB * HKV * NSPLIT, ATT_WAVES * 64, 0, stream>>>(K, V, ws);
  tail_kernel<<<KP * CG + CG, 256, 0, stream>>>(x, wo, wd, ws, (unsigned short*)d_out);
}
```

```cpp
#include <hip/hip_runtime.h>
#include <hip/hip_bf16.h>
#include <stdint.h>

constexpr int NB = 2, DM = 448, HQ = 16, HKV = 8, SK = 32768, DH = 256, FF = 1792;
constexpr int NSPLIT = 16;
constexpr int ATT_WAVES = 4;
constexpr int ATT_THREADS = ATT_WAVES * 64;
constexpr int NWG = NB * HKV * NSPLIT;
constexpr int CHUNK = 32;
constexpr int NCHUNK_BH = SK / CHUNK;
constexpr int WAVES_BH = NSPLIT * ATT_WAVES;
constexpr int CPW = NCHUNK_BH / WAVES_BH;
constexpr int CHUNK_BYTES = CHUNK * DH * 2;
constexpr int KPART = 8;
constexpr int KP_D = FF / 128;
constexpr int CG = DM / 64;
constexpr int NRED = 56;

constexpr size_t WS_Q = 0;
constexpr size_t WS_H = WS_Q + (size_t)KPART * NB * HQ * DH * 4;
constexpr size_t WS_G = WS_H + (size_t)2 * KPART * NB * FF * 4;
constexpr size_t G_PO = 0;
constexpr size_t G_ST = G_PO + (size_t)NWG * 512;
constexpr size_t G_OA = G_ST + (size_t)NWG * 4;
constexpr size_t G_OB = G_OA + (size_t)NWG * DM;
constexpr size_t G_END = G_OB + (size_t)KP_D * NB * DM;
static_assert(WS_G % 16 == 0 && G_END % 2 == 0, "alignment");

using u32x4 = __attribute__((ext_vector_type(4))) unsigned;
using f32x4 = __attribute__((ext_vector_type(4))) float;
using bf16x2 = __attribute__((ext_vector_type(2))) __bf16;
using rsrc_t = __amdgpu_buffer_rsrc_t;
using u64 = unsigned long long;

__device__ __forceinline__ float bf2f(unsigned short u) { return __uint_as_float(((unsigned)u) << 16); }
__device__ __forceinline__ float bflo(unsigned u) { return __uint_as_float(u << 16); }
__device__ __forceinline__ float bfhi(unsigned u) { return __uint_as_float(u & 0xffff0000u); }
__device__ __forceinline__ unsigned short f2bf(float x) { __bf16 h = (__bf16)x; return __builtin_bit_cast(unsigned short, h); }
__device__ __forceinline__ float bfr(float x) { __bf16 h = (__bf16)x; return (float)h; }
__device__ __forceinline__ float dot2(unsigned a, unsigned b, float acc) {
  return __builtin_amdgcn_fdot2_f32_bf16(__builtin_bit_cast(bf16x2, a), __builtin_bit_cast(bf16x2, b), acc, false);
}
template <int CTRL> __device__ __forceinline__ float dpp(float x) {
  return __builtin_bit_cast(float, __builtin_amdgcn_mov_dpp(__builtin_bit_cast(int, x), CTRL, 0xf, 0xf, true));
}
constexpr int XOR1 = 0xB1, XOR2 = 0x4E, XOR7 = 0x141, XOR8 = 0x128;

__device__ __forceinline__ void put_granule(u64* g, float v) {
  __hip_atomic_store(g, (1ull << 32) | (u64)__float_as_uint(v), __ATOMIC_RELAXED, __HIP_MEMORY_SCOPE_AGENT);
}
__device__ __forceinline__ u64 get_granule(u64* g) {
  return __hip_atomic_load(g, __ATOMIC_RELAXED, __HIP_MEMORY_SCOPE_AGENT);
}
constexpr unsigned SPIN_CAP = 1u << 22;

__device__ __forceinline__ float wave_sum(float v) {
#pragma unroll
  for (int o = 32; o > 0; o >>= 1) v += __shfl_xor(v, o, 64);
  return v;
}

__device__ __forceinline__ void layernorm_wave(float (&v)[7]) {
  float s = 0.f;
#pragma unroll
  for (int i = 0; i < 7; ++i) s += v[i];
  s = wave_sum(s);
  const float mean = s * (1.0f / DM);
  float ss = 0.f;
#pragma unroll
  for (int i = 0; i < 7; ++i) { const float d = v[i] - mean; ss += d * d; }
  ss = wave_sum(ss);
  const float var = ss * (1.0f / DM);
  const float mb = bfr(mean), vb = bfr(var);
  const float den = bfr(sqrtf(bfr(vb + 1e-5f)));
#pragma unroll
  for (int i = 0; i < 7; ++i) v[i] = bfr(bfr(v[i] - mb) / den);
}

__device__ __forceinline__ float gelu_tanh(float g) {
  const float u = 0.7978845608028654f * (g + 0.044715f * g * g * g);
  return 0.5f * g * (1.0f + tanhf(u));
}

constexpr int PREP_Q_WGS = (HQ * DH / 256) * KPART;
constexpr int PREP_F_WGS = (FF / 256) * KPART * 2;
constexpr int PREP_WGS = PREP_Q_WGS + PREP_F_WGS;

__global__ __launch_bounds__(256) void prep_kernel(const unsigned short* __restrict__ x,
                                                   const unsigned short* __restrict__ wq,
                                                   const unsigned short* __restrict__ wg,
                                                   const unsigned short* __restrict__ wu,
                                                   unsigned char* __restrict__ ws) {
  __shared__ __attribute__((aligned(16))) float lds[2 * DM + 4 * 2 * 256];
  float* xn = lds;
  float* red = lds + 2 * DM;
  const int t = threadIdx.x, wave = t >> 6, lane = t & 63;
  const int cl = t & 31, rg = t >> 5;
  const unsigned short* W;
  int ldw, kp;
  float* dst;
  if (blockIdx.x < PREP_Q_WGS) {
    const int cgp = blockIdx.x & 15; kp = blockIdx.x >> 4;
    W = wq + cgp * 256; ldw = HQ * DH;
    dst = reinterpret_cast<float*>(ws + WS_Q) + (size_t)kp * NB * HQ * DH + cgp * 256;
  } else {
    const int j = blockIdx.x - PREP_Q_WGS, mat = j / 56, r = j % 56, cgp = r % 7; kp = r / 7;
    W = (mat ? wu : wg) + cgp * 256; ldw = FF;
    dst = reinterpret_cast<float*>(ws + WS_H) + (size_t)(mat * KPART + kp) * NB * FF + cgp * 256;
  }
  const int row0 = kp * 56 + rg;
  float v[7];
  if (t < 128) {
#pragma unroll
    for (int i = 0; i < 7; ++i) v[i] = bf2f(x[wave * DM + lane + 64 * i]);
  }
  u32x4 w[7];
#pragma unroll
  for (int i = 0; i < 7; ++i) w[i] = *reinterpret_cast<const u32x4*>(W + (size_t)(row0 + 8 * i) * ldw + cl * 8);
  for (unsigned i = blockIdx.x * 256 + t; i < (unsigned)(G_END / 2); i += PREP_WGS * 256)
    reinterpret_cast<u32x4*>(ws + WS_G)[i] = u32x4{0u, 0u, 0u, 0u};
  if (t < 128) {
    layernorm_wave(v);
#pragma unroll
    for (int i = 0; i < 7; ++i) xn[wave * DM + lane + 64 * i] = v[i];
  }
  __syncthreads();
  float acc[2][8];
#pragma unroll
  for (int b = 0; b < 2; ++b)
#pragma unroll
    for (int k = 0; k < 8; ++k) acc[b][k] = 0.f;
#pragma unroll
  for (int i = 0; i < 7; ++i) {
    const float x0 = xn[row0 + 8 * i], x1 = xn[DM + row0 + 8 * i];
#pragma unroll
    for (int k = 0; k < 4; ++k) {
      const float lo = bflo(w[i][k]), hi = bfhi(w[i][k]);
      acc[0][2 * k] = fmaf(x0, lo, acc[0][2 * k]); acc[0][2 * k + 1] = fmaf(x0, hi, acc[0][2 * k + 1]);
      acc[1][2 * k] = fmaf(x1, lo, acc[1][2 * k]); acc[1][2 * k + 1] = fmaf(x1, hi, acc[1][2 * k + 1]);
    }
  }
#pragma unroll
  for (int b = 0; b < 2; ++b)
#pragma unroll
    for (int k = 0; k < 8; ++k) acc[b][k] += __shfl_xor(acc[b][k], 32, 64);
  if (lane < 32) {
#pragma unroll
    for (int b = 0; b < 2; ++b) {
      float* r = red + (wave * 2 + b) * 256 + lane * 8;
      *reinterpret_cast<f32x4*>(r) = f32x4{acc[b][0], acc[b][1], acc[b][2], acc[b][3]};
      *reinterpret_cast<f32x4*>(r + 4) = f32x4{acc[b][4], acc[b][5], acc[b][6], acc[b][7]};
    }
  }
  __syncthreads();
#pragma unroll
  for (int b = 0; b < 2; ++b) {
    const float* r = red + b * 256 + t;
    dst[(size_t)b * ldw + t] = (r[0] + r[512]) + (r[1024] + r[1536]);
  }
}

constexpr int KV_AUX = 2;
__device__ __forceinline__ u32x4 ldkv(rsrc_t r, unsigned voff, unsigned soff) {
  return __builtin_bit_cast(u32x4, __builtin_amdgcn_raw_buffer_load_b128(r, voff, soff, KV_AUX));
}

#define PIN16(a) asm volatile("" : "+v"(a[0]), "+v"(a[1]), "+v"(a[2]), "+v"(a[3]), "+v"(a[4]), "+v"(a[5]), "+v"(a[6]), "+v"(a[7]), \
    "+v"(a[8]), "+v"(a[9]), "+v"(a[10]), "+v"(a[11]), "+v"(a[12]), "+v"(a[13]), "+v"(a[14]), "+v"(a[15]) :: "memory")

constexpr int L_P = 0;
constexpr int L_MRG = L_P + ATT_WAVES * 64;
constexpr int L_MLS = L_MRG + ATT_WAVES * 2 * DH;
constexpr int L_WO = L_MLS + ATT_WAVES * 4;
constexpr int L_G = L_WO + 32 * DM / 2;
constexpr int L_END = L_G + ATT_WAVES * 512;

__global__ __launch_bounds__(ATT_THREADS, 1) void attn_kernel(const unsigned short* __restrict__ Kc,
                                                              const unsigned short* __restrict__ Vc,
                                                              const unsigned short* __restrict__ x,
                                                              const unsigned short* __restrict__ wo,
                                                              const unsigned short* __restrict__ wd,
                                                              unsigned char* __restrict__ ws,
                                                              unsigned short* __restrict__ out) {
  __shared__ __attribute__((aligned(16))) float lds[L_END];
  float* plds = lds + L_P;
  float* mrg = lds + L_MRG;
  float* mls = lds + L_MLS;
  const int t = threadIdx.x, lane = t & 63, wave = __builtin_amdgcn_readfirstlane(t >> 6);
  const int bh = blockIdx.x >> 4, split = blockIdx.x & 15;
  const int b = bh >> 3, kvh = bh & 7;
  u64* G = reinterpret_cast<u64*>(ws + WS_G);
  const int c = lane & 31;
  const size_t base = (size_t)bh * SK * DH;
  const rsrc_t kr = __builtin_amdgcn_make_buffer_rsrc((void*)(Kc + base), 0, SK * DH * 2, 0x00020000);
  const rsrc_t vr = __builtin_amdgcn_make_buffer_rsrc((void*)(Vc + base), 0, SK * DH * 2, 0x00020000);
  f32x4 qa[4];
  {
    const float* qg = reinterpret_cast<const float*>(ws + WS_Q) + (size_t)(2 * wave + (lane >> 5)) * NB * HQ * DH +
                      (size_t)b * HQ * DH + kvh * 512 + 8 * c;
    qa[0] = *reinterpret_cast<const f32x4*>(qg); qa[1] = *reinterpret_cast<const f32x4*>(qg + 4);
    qa[2] = *reinterpret_cast<const f32x4*>(qg + 256); qa[3] = *reinterpret_cast<const f32x4*>(qg + 260);
  }
  u32x4 wv[7];
  {
    const u32x4* wsrc = reinterpret_cast<const u32x4*>(wo + (size_t)(kvh * 512 + split * 32) * DM);
#pragma unroll
    for (int i = 0; i < 7; ++i) wv[i] = wsrc[t + 256 * i];
  }
  const bool btask = (blockIdx.x & 1) == 0 && (blockIdx.x >> 1) < KP_D * CG;
  const int bkp = (blockIdx.x >> 1) / CG, bcg = (blockIdx.x >> 1) % CG;
  u32x4 wdt[4];
  float gu[2 * KPART];
  if (btask) {
    const int cq = t & 7, rg = t >> 3;
#pragma unroll
    for (int i = 0; i < 4; ++i)
      wdt[i] = *reinterpret_cast<const u32x4*>(wd + (size_t)(bkp * 128 + rg + 32 * i) * DM + bcg * 64 + cq * 8);
    const float* gp = reinterpret_cast<const float*>(ws + WS_H) + (size_t)(t >> 7) * FF + bkp * 128 + (t & 127);
#pragma unroll
    for (int k8 = 0; k8 < 2 * KPART; ++k8) gu[k8] = gp[(size_t)k8 * NB * FF];
  }
  const unsigned rank = split * ATT_WAVES + wave;
  unsigned cur = rank * CPW;
  const unsigned last = cur + CPW - 1;
  u32x4 kb[16], vb[16];
  __builtin_amdgcn_sched_barrier(0);
  {
    const unsigned vo = cur * CHUNK_BYTES + lane * 16;
#pragma unroll
    for (int i = 0; i < 16; ++i) kb[i] = ldkv(kr, vo + (i & 3) * 1024, (i >> 2) * 4096);
    __builtin_amdgcn_sched_barrier(0);
#pragma unroll
    for (int i = 0; i < 16; ++i) vb[i] = ldkv(vr, vo + (i & 3) * 1024, (i >> 2) * 4096);
    __builtin_amdgcn_sched_barrier(0);
  }
  u32x4 q0, q1;
  {
    float* qs = lds + L_G;
#pragma unroll
    for (int u = 0; u < 4; ++u)
#pragma unroll
      for (int k = 0; k < 4; ++k) {
        auto r = __builtin_amdgcn_permlane32_swap(__float_as_uint(qa[u][k]), __float_as_uint(qa[u][k]), false, false);
        qa[u][k] = __uint_as_float(r[0]) + __uint_as_float(r[1]);
      }
    if (lane < 32) {
      *reinterpret_cast<f32x4*>(qs + wave * 512 + 8 * c) = qa[0];
      *reinterpret_cast<f32x4*>(qs + wave * 512 + 8 * c + 4) = qa[1];
      *reinterpret_cast<f32x4*>(qs + wave * 512 + 256 + 8 * c) = qa[2];
      *reinterpret_cast<f32x4*>(qs + wave * 512 + 256 + 8 * c + 4) = qa[3];
    }
    u32x4* wl = reinterpret_cast<u32x4*>(lds + L_WO);
#pragma unroll
    for (int i = 0; i < 7; ++i) wl[t + 256 * i] = wv[i];
    float* vec = mrg;
    if (btask) {
      float g = 0.f, u = 0.f;
#pragma unroll
      for (int k8 = 0; k8 < KPART; ++k8) { g += gu[k8]; u += gu[KPART + k8]; }
      vec[t] = bfr(bfr(gelu_tanh(bfr(g))) * bfr(u));
    }
    __syncthreads();
#pragma unroll
    for (int u = 0; u < 4; ++u) {
      const float* qp = qs + (u >> 1) * 256 + 8 * c + (u & 1) * 4;
      qa[u] = (*reinterpret_cast<const f32x4*>(qp) + *reinterpret_cast<const f32x4*>(qp + 512)) +
              (*reinterpret_cast<const f32x4*>(qp + 1024) + *reinterpret_cast<const f32x4*>(qp + 1536));
    }
#pragma unroll
    for (int k = 0; k < 4; ++k) {
      q0[k] = (unsigned)f2bf(qa[k >> 1][(2 * k) & 3]) | ((unsigned)f2bf(qa[k >> 1][(2 * k + 1) & 3]) << 16);
      q1[k] = (unsigned)f2bf(qa[2 + (k >> 1)][(2 * k) & 3]) | ((unsigned)f2bf(qa[2 + (k >> 1)][(2 * k + 1) & 3]) << 16);
    }
    if (btask) {
      const int rg = t >> 3;
      float* red = mrg + 256;
      float a2[2][8];
#pragma unroll
      for (int bb = 0; bb < 2; ++bb)
#pragma unroll
        for (int k = 0; k < 8; ++k) a2[bb][k] = 0.f;
#pragma unroll
      for (int i = 0; i < 4; ++i) {
        const float x0 = vec[rg + 32 * i], x1 = vec[128 + rg + 32 * i];
#pragma unroll
        for (int k = 0; k < 4; ++k) {
          const float lo = bflo(wdt[i][k]), hi = bfhi(wdt[i][k]);
          a2[0][2 * k] = fmaf(x0, lo, a2[0][2 * k]); a2[0][2 * k + 1] = fmaf(x0, hi, a2[0][2 * k + 1]);
          a2[1][2 * k] = fmaf(x1, lo, a2[1][2 * k]); a2[1][2 * k + 1] = fmaf(x1, hi, a2[1][2 * k + 1]);
        }
      }
#pragma unroll
      for (int bb = 0; bb < 2; ++bb)
#pragma unroll
        for (int k = 0; k < 8; ++k) {
          float v = a2[bb][k];
          v += __shfl_xor(v, 8, 64); v += __shfl_xor(v, 16, 64); v += __shfl_xor(v, 32, 64);
          a2[bb][k] = v;
        }
      if (lane < 8) {
#pragma unroll
        for (int bb = 0; bb < 2; ++bb)
#pragma unroll
          for (int k = 0; k < 8; ++k) red[(wave * 2 + bb) * 64 + lane * 8 + k] = a2[bb][k];
      }
      __syncthreads();
      if (t < 128) {
        const int bb = t >> 6, cc = t & 63;
        const float v = (red[(0 * 2 + bb) * 64 + cc] + red[(1 * 2 + bb) * 64 + cc]) + (red[(2 * 2 + bb) * 64 + cc] + red[(3 * 2 + bb) * 64 + cc]);
        put_granule(G + G_OB + (size_t)(bkp * 2 + bb) * DM + bcg * 64 + cc, v);
      }
    }
  }
  __builtin_amdgcn_sched_barrier(0);
  float acc[16];
#pragma unroll
  for (int k = 0; k < 16; ++k) acc[k] = 0.f;
  float m = -INFINITY, l = 0.f;
  float* pw = plds + wave * 64;
  constexpr float SC = 0.0625f * 1.4426950408889634f;
  bool more;
  do {
    float xx[16];
#pragma unroll
    for (int i = 0; i < 16; ++i) {
      float s0 = 0.f, s1 = 0.f;
#pragma unroll
      for (int k = 0; k < 4; ++k) { s0 = dot2(kb[i][k], q0[k], s0); s1 = dot2(kb[i][k], q1[k], s1); }
      auto r = __builtin_amdgcn_permlane16_swap(__float_as_uint(s0), __float_as_uint(s1), false, false);
      xx[i] = __uint_as_float(r[0]) + __uint_as_float(r[1]);
    }
    PIN16(xx);
    more = cur < last;
    const unsigned von = (more ? (cur + 1) : (unsigned)NCHUNK_BH) * CHUNK_BYTES + lane * 16;
#pragma unroll
    for (int i = 0; i < 16; ++i) kb[i] = ldkv(kr, von + (i & 3) * 1024, (i >> 2) * 4096);
    asm volatile("" ::: "memory");
#define TR_STEP(HALF, CTRL, BIT)                                                       \
  _Pragma("unroll") for (int h = 0; h < (HALF); ++h) {                                 \
    const bool up = (lane & (BIT)) != 0;                                               \
    const float keep = up ? xx[h + (HALF)] : xx[h], send = up ? xx[h] : xx[h + (HALF)]; \
    xx[h] = keep + dpp<CTRL>(send);                                                    \
  }
    TR_STEP(8, XOR8, 8) TR_STEP(4, XOR7, 4) TR_STEP(2, XOR2, 2) TR_STEP(1, XOR1, 1)
#undef TR_STEP
    const float s = bfr(xx[0]) * SC;
    float pm = s;
    pm = fmaxf(pm, dpp<XOR1>(pm)); pm = fmaxf(pm, dpp<XOR2>(pm)); pm = fmaxf(pm, dpp<XOR7>(pm)); pm = fmaxf(pm, dpp<XOR8>(pm));
    {
      auto r = __builtin_amdgcn_permlane32_swap(__float_as_uint(pm), __float_as_uint(pm), false, false);
      pm = fmaxf(__uint_as_float(r[0]), __uint_as_float(r[1]));
    }
    const float mn = fmaxf(m, pm);
    const float alpha = __builtin_amdgcn_exp2f(m - mn);
    const float p = __builtin_amdgcn_exp2f(s - mn);
    l = fmaf(l, alpha, p);
    m = mn;
    const float a0 = __uint_as_float(__builtin_amdgcn_readlane(__float_as_uint(alpha), 0));
    const float a1 = __uint_as_float(__builtin_amdgcn_readlane(__float_as_uint(alpha), 16));
#pragma unroll
    for (int k = 0; k < 8; ++k) { acc[k] *= a0; acc[8 + k] *= a1; }
    pw[lane] = p;
    asm volatile("s_waitcnt lgkmcnt(0)" ::: "memory");
    const float* prd = pw + (lane >> 5) * 32;
#pragma unroll
    for (int u = 0; u < 4; ++u) {
      const f32x4 pa = *reinterpret_cast<const f32x4*>(prd + 4 * u);
      const f32x4 pb = *reinterpret_cast<const f32x4*>(prd + 16 + 4 * u);
      asm volatile("" : "+v"(vb[4 * u]), "+v"(vb[4 * u + 1]), "+v"(vb[4 * u + 2]), "+v"(vb[4 * u + 3]));
#pragma unroll
      for (int j = 0; j < 4; ++j) {
        const int i = 4 * u + j;
        const float p0 = pa[j], p1 = pb[j];
#pragma unroll
        for (int k = 0; k < 4; ++k) {
          const float lo = bflo(vb[i][k]), hi = bfhi(vb[i][k]);
          acc[2 * k] = fmaf(p0, lo, acc[2 * k]); acc[2 * k + 1] = fmaf(p0, hi, acc[2 * k + 1]);
          acc[8 + 2 * k] = fmaf(p1, lo, acc[8 + 2 * k]); acc[8 + 2 * k + 1] = fmaf(p1, hi, acc[8 + 2 * k + 1]);
        }
      }
    }
    PIN16(acc);
#pragma unroll
    for (int i = 0; i < 16; ++i) vb[i] = ldkv(vr, von + (i & 3) * 1024, (i >> 2) * 4096);
    asm volatile("" ::: "memory");
    ++cur;
  } while (more);
#pragma unroll
  for (int k = 0; k < 16; ++k) {
    auto r = __builtin_amdgcn_permlane32_swap(__float_as_uint(acc[k]), __float_as_uint(acc[k]), false, false);
    acc[k] = __uint_as_float(r[0]) + __uint_as_float(r[1]);
  }
  l += dpp<XOR1>(l); l += dpp<XOR2>(l); l += dpp<XOR7>(l); l += dpp<XOR8>(l);
  {
    auto r = __builtin_amdgcn_permlane32_swap(__float_as_uint(l), __float_as_uint(l), false, false);
    l = __uint_as_float(r[0]) + __uint_as_float(r[1]);
  }
  if (lane < 32) {
#pragma unroll
    for (int h = 0; h < 2; ++h) {
      *reinterpret_cast<f32x4*>(mrg + (wave * 2 + h) * DH + 8 * c) = f32x4{acc[8 * h + 0], acc[8 * h + 1], acc[8 * h + 2], acc[8 * h + 3]};
      *reinterpret_cast<f32x4*>(mrg + (wave * 2 + h) * DH + 8 * c + 4) = f32x4{acc[8 * h + 4], acc[8 * h + 5], acc[8 * h + 6], acc[8 * h + 7]};
    }
    if ((lane & 15) == 0) { const int h = lane >> 4; mls[(wave * 2 + h) * 2] = m; mls[(wave * 2 + h) * 2 + 1] = l; }
  }
  __syncthreads();
#pragma unroll
  for (int h = 0; h < 2; ++h) {
    float M = -INFINITY;
#pragma unroll
    for (int w = 0; w < ATT_WAVES; ++w) M = fmaxf(M, mls[(w * 2 + h) * 2]);
    float L = 0.f, o = 0.f;
#pragma unroll
    for (int w = 0; w < ATT_WAVES; ++w) {
      const float wt = __builtin_amdgcn_exp2f(mls[(w * 2 + h) * 2] - M);
      L = fmaf(wt, mls[(w * 2 + h) * 2 + 1], L);
      o = fmaf(wt, mrg[(w * 2 + h) * DH + t], o);
    }
    put_granule(G + G_PO + (size_t)blockIdx.x * 512 + h * 256 + t, o);
    if (t == 0) { put_granule(G + G_ST + (size_t)blockIdx.x * 4 + h * 2, M); put_granule(G + G_ST + (size_t)blockIdx.x * 4 + h * 2 + 1, L); }
  }
  float* gM = lds + L_G;
  float* gL = gM + 16;
  float* gO = gM + 32;
  float* ov = gO + 512;
  {
    const int hh = split >> 3, d0 = (split & 7) * 32;
    const int s = t >> 4, j = t & 15;
    u64* gp = G + G_PO + (size_t)(bh * NSPLIT + s) * 512 + hh * 256 + d0 + 2 * j;
    u64* gs = G + G_ST + (size_t)(bh * NSPLIT + s) * 4 + hh * 2;
    u64 g0, g1, g2, g3;
    for (unsigned spin = 0; spin < SPIN_CAP; ++spin) {
      g0 = get_granule(gp); g1 = get_granule(gp + 1); g2 = get_granule(gs); g3 = get_granule(gs + 1);
      const bool ok = ((g0 & g1 & g2 & g3) >> 32) == 1ull;
      if (__all(ok)) break;
      __builtin_amdgcn_s_sleep(2);
    }
    gO[s * 32 + 2 * j] = __uint_as_float((unsigned)g0);
    gO[s * 32 + 2 * j + 1] = __uint_as_float((unsigned)g1);
    if (j == 0) { gM[s] = __uint_as_float((unsigned)g2); gL[s] = __uint_as_float((unsigned)g3); }
  }
  __syncthreads();
  if (t < 32) {
    float M = -INFINITY;
#pragma unroll
    for (int s = 0; s < NSPLIT; ++s) M = fmaxf(M, gM[s]);
    float L = 0.f, o = 0.f;
#pragma unroll
    for (int s = 0; s < NSPLIT; ++s) {
      const float wt = __builtin_amdgcn_exp2f(gM[s] - M);
      L = fmaf(wt, gL[s], L); o = fmaf(wt, gO[s * 32 + t], o);
    }
    ov[t] = bfr(o / L);
  }
  __syncthreads();
  if (t < DM / 2) {
    const unsigned* wl = reinterpret_cast<const unsigned*>(lds + L_WO) + t;
    float y0 = 0.f, y1 = 0.f;
#pragma unroll
    for (int d = 0; d < 32; ++d) {
      const unsigned w = wl[d * (DM / 2)];
      const float od = ov[d];
      y0 = fmaf(od, bflo(w), y0); y1 = fmaf(od, bfhi(w), y1);
    }
    u64* g = G + G_OA + (size_t)blockIdx.x * DM + 2 * t;
    put_granule(g, y0); put_granule(g + 1, y1);
  }
  if (blockIdx.x < NRED) {
    const int br = blockIdx.x / 28, c0 = (blockIdx.x % 28) * 16;
    const int col = t & 15, rgp = t >> 4;
    u64* ga = G + G_OA + (size_t)(br * 128 + rgp * 8) * DM + c0 + col;
    u64* gb = G + G_OB + (size_t)((rgp < KP_D ? rgp : 0) * 2 + br) * DM + c0 + col;
    u64 va[8], vbq;
    for (unsigned spin = 0; spin < SPIN_CAP; ++spin) {
      u64 all = ~0ull;
#pragma unroll
      for (int i = 0; i < 8; ++i) { va[i] = get_granule(ga + (size_t)i * DM); all &= va[i]; }
      vbq = get_granule(gb); all &= vbq;
      if (__all((all >> 32) == 1ull)) break;
      __builtin_amdgcn_s_sleep(2);
    }
    float A = 0.f;
#pragma unroll
    for (int i = 0; i < 8; ++i) A += __uint_as_float((unsigned)va[i]);
    float* ra = mrg;
    float* rb = mrg + 256;
    ra[rgp * 16 + col] = A;
    rb[rgp * 16 + col] = rgp < KP_D ? __uint_as_float((unsigned)vbq) : 0.f;
    __syncthreads();
    if (t < 16) {
      float As = 0.f, Bs = 0.f;
#pragma unroll
      for (int r = 0; r < 16; ++r) { As += ra[r * 16 + t]; Bs += rb[r * 16 + t]; }
      const float xv = bf2f(x[br * DM + c0 + t]);
      out[br * DM + c0 + t] = f2bf(bfr(xv + bfr(As)) + bfr(Bs));
    }
  }
}

extern "C" void kernel_launch(void* const* d_in, const int* in_sizes, int n_in, void* d_out, int out_size, void* d_ws,
                              size_t ws_size, hipStream_t stream) {
  const unsigned short* x = (const unsigned short*)d_in[0];
  const unsigned short* K = (const unsigned short*)d_in[1];
  const unsigned short* V = (const unsigned short*)d_in[2];
  const unsigned short* wq = (const unsigned short*)d_in[3];
  const unsigned short* wo = (const unsigned short*)d_in[4];
  const unsigned short* wg = (const unsigned short*)d_in[6];
  const unsigned short* wu = (const unsigned short*)d_in[7];
  const unsigned short* wd = (const unsigned short*)d_in[8];
  unsigned char* ws = (unsigned char*)d_ws;
  prep_kernel<<<PREP_WGS, 256, 0, stream>>>(x, wq, wg, wu, ws);
  attn_kernel<<<NWG, ATT_THREADS, 0, stream>>>(K, V, x, wo, wd, ws, (unsigned short*)d_out);
}
```

```cpp
#include <hip/hip_runtime.h>
#include <hip/hip_bf16.h>
#include <stdint.h>


constexpr int NB = 2, DM = 448, HQ = 16, HKV = 8, SK = 32768, DH = 256, FF = 1792;
constexpr int NSPLIT = 16;
constexpr int ATT_WAVES = 4;
constexpr int ATT_THREADS = ATT_WAVES * 64;
constexpr int NWG = NB * HKV * NSPLIT;
constexpr int CHUNK = 32;
constexpr int NCHUNK_BH = SK / CHUNK;
constexpr int WAVES_BH = NSPLIT * ATT_WAVES;
constexpr int CPW = NCHUNK_BH / WAVES_BH;
constexpr int CHUNK_BYTES = CHUNK * DH * 2;
constexpr int KPART = 8;
constexpr int KP_D = FF / 128;
constexpr int CG = DM / 64;
constexpr int NRED = 56;

constexpr size_t WS_Q = 0;
constexpr size_t WS_H = WS_Q + (size_t)KPART * NB * HQ * DH * 4;
constexpr size_t WS_G = WS_H + (size_t)2 * KPART * NB * FF * 4;
constexpr size_t G_PO = 0;
constexpr size_t G_ST = G_PO + (size_t)NWG * 512;
constexpr size_t G_OA = G_ST + (size_t)NWG * 4;
constexpr size_t G_OB = G_OA + (size_t)NWG * DM;
constexpr size_t G_END = G_OB + (size_t)KP_D * NB * DM;
static_assert(WS_G % 16 == 0 && G_END % 2 == 0, "alignment");

using u32x4 = __attribute__((ext_vector_type(4))) unsigned;
using f32x4 = __attribute__((ext_vector_type(4))) float;
using bf16x2 = __attribute__((ext_vector_type(2))) __bf16;
using rsrc_t = __amdgpu_buffer_rsrc_t;
using u64 = unsigned long long;

__device__ __forceinline__ float bf2f(unsigned short u) { return __uint_as_float(((unsigned)u) << 16); }
__device__ __forceinline__ float bflo(unsigned u) { return __uint_as_float(u << 16); }
__device__ __forceinline__ float bfhi(unsigned u) { return __uint_as_float(u & 0xffff0000u); }
__device__ __forceinline__ unsigned short f2bf(float x) { __bf16 h = (__bf16)x; return __builtin_bit_cast(unsigned short, h); }
__device__ __forceinline__ float bfr(float x) { __bf16 h = (__bf16)x; return (float)h; }
__device__ __forceinline__ float dot2(unsigned a, unsigned b, float acc) {
  return __builtin_amdgcn_fdot2_f32_bf16(__builtin_bit_cast(bf16x2, a), __builtin_bit_cast(bf16x2, b), acc, false);
}
template <int CTRL> __device__ __forceinline__ float dpp(float x) {
  return __builtin_bit_cast(float, __builtin_amdgcn_mov_dpp(__builtin_bit_cast(int, x), CTRL, 0xf, 0xf, true));
}
constexpr int XOR1 = 0xB1, XOR2 = 0x4E, XOR7 = 0x141, XOR8 = 0x128;

__device__ __forceinline__ void put_granule(u64* g, float v) {
  __hip_atomic_store(g, (1ull << 32) | (u64)__float_as_uint(v), __ATOMIC_RELAXED, __HIP_MEMORY_SCOPE_AGENT);
}
__device__ __forceinline__ u64 get_granule(u64* g) {
  return __hip_atomic_load(g, __ATOMIC_RELAXED, __HIP_MEMORY_SCOPE_AGENT);
}
constexpr unsigned SPIN_CAP = 1u << 22;

__device__ __forceinline__ float wave_sum(float v) {
#pragma unroll
  for (int o = 32; o > 0; o >>= 1) v += __shfl_xor(v, o, 64);
  return v;
}

__device__ __forceinline__ void layernorm_wave(float (&v)[7]) {
  float s = 0.f;
#pragma unroll
  for (int i = 0; i < 7; ++i) s += v[i];
  s = wave_sum(s);
  const float mean = s * (1.0f / DM);
  float ss = 0.f;
#pragma unroll
  for (int i = 0; i < 7; ++i) { const float d = v[i] - mean; ss += d * d; }
  ss = wave_sum(ss);
  const float var = ss * (1.0f / DM);
  const float mb = bfr(mean), vb = bfr(var);
  const float den = bfr(sqrtf(bfr(vb + 1e-5f)));
#pragma unroll
  for (int i = 0; i < 7; ++i) v[i] = bfr(bfr(v[i] - mb) / den);
}

__device__ __forceinline__ float gelu_tanh(float g) {
  const float u = 0.7978845608028654f * (g + 0.044715f * g * g * g);
  return 0.5f * g * (1.0f + tanhf(u));
}

constexpr int PREP_Q_WGS = (HQ * DH / 256) * KPART;
constexpr int PREP_F_WGS = (FF / 256) * KPART * 2;
constexpr int PREP_WGS = PREP_Q_WGS + PREP_F_WGS;

__global__ __launch_bounds__(256) void prep_kernel(const unsigned short* __restrict__ x,
                                                   const unsigned short* __restrict__ wq,
                                                   const unsigned short* __restrict__ wg,
                                                   const unsigned short* __restrict__ wu,
                                                   unsigned char* __restrict__ ws) {
  __shared__ __attribute__((aligned(16))) float lds[2 * DM + 4 * 2 * 256];
  float* xn = lds;
  float* red = lds + 2 * DM;
  const int t = threadIdx.x, wave = t >> 6, lane = t & 63;
  const int cl = t & 31, rg = t >> 5;
  const unsigned short* W;
  int ldw, kp;
  float* dst;
  if (blockIdx.x < PREP_Q_WGS) {
    const int cgp = blockIdx.x & 15; kp = blockIdx.x >> 4;
    W = wq + cgp * 256; ldw = HQ * DH;
    dst = reinterpret_cast<float*>(ws + WS_Q) + (size_t)kp * NB * HQ * DH + cgp * 256;
  } else {
    const int j = blockIdx.x - PREP_Q_WGS, mat = j / 56, r = j % 56, cgp = r % 7; kp = r / 7;
    W = (mat ? wu : wg) + cgp * 256; ldw = FF;
    dst = reinterpret_cast<float*>(ws + WS_H) + (size_t)(mat * KPART + kp) * NB * FF + cgp * 256;
  }
  const int row0 = kp * 56 + rg;
  float v[7];
  if (t < 128) {
#pragma unroll
    for (int i = 0; i < 7; ++i) v[i] = bf2f(x[wave * DM + lane + 64 * i]);
  }
  u32x4 w[7];
#pragma unroll
  for (int i = 0; i < 7; ++i) w[i] = *reinterpret_cast<const u32x4*>(W + (size_t)(row0 + 8 * i) * ldw + cl * 8);
  for (unsigned i = blockIdx.x * 256 + t; i < (unsigned)(G_END / 2); i += PREP_WGS * 256)
    reinterpret_cast<u32x4*>(ws + WS_G)[i] = u32x4{0u, 0u, 0u, 0u};
  if (t < 128) {
    layernorm_wave(v);
#pragma unroll
    for (int i = 0; i < 7; ++i) xn[wave * DM + lane + 64 * i] = v[i];
  }
  __syncthreads();
  float acc[2][8];
#pragma unroll
  for (int b = 0; b < 2; ++b)
#pragma unroll
    for (int k = 0; k < 8; ++k) acc[b][k] = 0.f;
#pragma unroll
  for (int i = 0; i < 7; ++i) {
    const float x0 = xn[row0 + 8 * i], x1 = xn[DM + row0 + 8 * i];
#pragma unroll
    for (int k = 0; k < 4; ++k) {
      const float lo = bflo(w[i][k]), hi = bfhi(w[i][k]);
      acc[0][2 * k] = fmaf(x0, lo, acc[0][2 * k]); acc[0][2 * k + 1] = fmaf(x0, hi, acc[0][2 * k + 1]);
      acc[1][2 * k] = fmaf(x1, lo, acc[1][2 * k]); acc[1][2 * k + 1] = fmaf(x1, hi, acc[1][2 * k + 1]);
    }
  }
#pragma unroll
  for (int b = 0; b < 2; ++b)
#pragma unroll
    for (int k = 0; k < 8; ++k) acc[b][k] += __shfl_xor(acc[b][k], 32, 64);
  if (lane < 32) {
#pragma unroll
    for (int b = 0; b < 2; ++b) {
      float* r = red + (wave * 2 + b) * 256 + lane * 8;
      *reinterpret_cast<f32x4*>(r) = f32x4{acc[b][0], acc[b][1], acc[b][2], acc[b][3]};
      *reinterpret_cast<f32x4*>(r + 4) = f32x4{acc[b][4], acc[b][5], acc[b][6], acc[b][7]};
    }
  }
  __syncthreads();
#pragma unroll
  for (int b = 0; b < 2; ++b) {
    const float* r = red + b * 256 + t;
    dst[(size_t)b * ldw + t] = (r[0] + r[512]) + (r[1024] + r[1536]);
  }
}

constexpr int KV_AUX = 2;
__device__ __forceinline__ u32x4 ldkv(rsrc_t r, unsigned voff, unsigned soff) {
  return __builtin_bit_cast(u32x4, __builtin_amdgcn_raw_buffer_load_b128(r, voff, soff, KV_AUX));
}

#define PIN16(a) asm volatile("" : "+v"(a[0]), "+v"(a[1]), "+v"(a[2]), "+v"(a[3]), "+v"(a[4]), "+v"(a[5]), "+v"(a[6]), "+v"(a[7]), \
    "+v"(a[8]), "+v"(a[9]), "+v"(a[10]), "+v"(a[11]), "+v"(a[12]), "+v"(a[13]), "+v"(a[14]), "+v"(a[15]) :: "memory")

constexpr int L_P = 0;
constexpr int L_MRG = L_P + ATT_WAVES * 64;
constexpr int L_MLS = L_MRG + ATT_WAVES * 2 * DH;
constexpr int L_WO = L_MLS + ATT_WAVES * 4;
constexpr int L_G = L_WO + 32 * DM / 2;
constexpr int L_B = L_G + ATT_WAVES * 512;
constexpr int L_END = L_B + 256 + 512;

__global__ __launch_bounds__(ATT_THREADS, 1) void attn_kernel(const unsigned short* __restrict__ Kc,
                                                              const unsigned short* __restrict__ Vc,
                                                              const unsigned short* __restrict__ x,
                                                              const unsigned short* __restrict__ wo,
                                                              const unsigned short* __restrict__ wd,
                                                              unsigned char* __restrict__ ws,
                                                              unsigned short* __restrict__ out) {
  __shared__ __attribute__((aligned(16))) float lds[L_END];
  float* plds = lds + L_P;
  float* mrg = lds + L_MRG;
  float* mls = lds + L_MLS;
  const int t = threadIdx.x, lane = t & 63, wave = __builtin_amdgcn_readfirstlane(t >> 6);
  const int bh = blockIdx.x >> 4, split = blockIdx.x & 15;
  const int b = bh >> 3, kvh = bh & 7;
  u64* G = reinterpret_cast<u64*>(ws + WS_G);
  const int c = lane & 31;
  const size_t base = (size_t)bh * SK * DH;
  const rsrc_t kr = __builtin_amdgcn_make_buffer_rsrc((void*)(Kc + base), 0, SK * DH * 2, 0x00020000);
  const rsrc_t vr = __builtin_amdgcn_make_buffer_rsrc((void*)(Vc + base), 0, SK * DH * 2, 0x00020000);
  f32x4 qa[4];
  {
    const float* qg = reinterpret_cast<const float*>(ws + WS_Q) + (size_t)(2 * wave + (lane >> 5)) * NB * HQ * DH +
                      (size_t)b * HQ * DH + kvh * 512 + 8 * c;
    qa[0] = *reinterpret_cast<const f32x4*>(qg); qa[1] = *reinterpret_cast<const f32x4*>(qg + 4);
    qa[2] = *reinterpret_cast<const f32x4*>(qg + 256); qa[3] = *reinterpret_cast<const f32x4*>(qg + 260);
  }
  u32x4 wv[7];
  {
    const u32x4* wsrc = reinterpret_cast<const u32x4*>(wo + (size_t)(kvh * 512 + split * 32) * DM);
#pragma unroll
    for (int i = 0; i < 7; ++i) wv[i] = wsrc[t + 256 * i];
  }
  const bool btask = (blockIdx.x & 1) == 0 && (blockIdx.x >> 1) < KP_D * CG;
  const int bkp = (blockIdx.x >> 1) / CG, bcg = (blockIdx.x >> 1) % CG;
  u32x4 wdt[4];
  float gu[2 * KPART];
  if (btask) {
    const int cq = t & 7, rg = t >> 3;
#pragma unroll
    for (int i = 0; i < 4; ++i)
      wdt[i] = *reinterpret_cast<const u32x4*>(wd + (size_t)(bkp * 128 + rg + 32 * i) * DM + bcg * 64 + cq * 8);
    const float* gp = reinterpret_cast<const float*>(ws + WS_H) + (size_t)(t >> 7) * FF + bkp * 128 + (t & 127);
#pragma unroll
    for (int k8 = 0; k8 < 2 * KPART; ++k8) gu[k8] = gp[(size_t)k8 * NB * FF];
  }
  const unsigned rank = split * ATT_WAVES + wave;
  unsigned cur = rank * CPW;
  const unsigned last = cur + CPW - 1;
  u32x4 kb[16], vb[16];
  __builtin_amdgcn_sched_barrier(0);
  {
    const unsigned vo = cur * CHUNK_BYTES + lane * 16;
#pragma unroll
    for (int i = 0; i < 16; ++i) kb[i] = ldkv(kr, vo + (i & 3) * 1024, (i >> 2) * 4096);
    __builtin_amdgcn_sched_barrier(0);
#pragma unroll
    for (int i = 0; i < 16; ++i) vb[i] = ldkv(vr, vo + (i & 3) * 1024, (i >> 2) * 4096);
    __builtin_amdgcn_sched_barrier(0);
  }
  u32x4 q0, q1;
  {
    float* qs = lds + L_G;
#pragma unroll
    for (int u = 0; u < 4; ++u)
#pragma unroll
      for (int k = 0; k < 4; ++k) {
        auto r = __builtin_amdgcn_permlane32_swap(__float_as_uint(qa[u][k]), __float_as_uint(qa[u][k]), false, false);
        qa[u][k] = __uint_as_float(r[0]) + __uint_as_float(r[1]);
      }
    if (lane < 32) {
      *reinterpret_cast<f32x4*>(qs + wave * 512 + 8 * c) = qa[0];
      *reinterpret_cast<f32x4*>(qs + wave * 512 + 8 * c + 4) = qa[1];
      *reinterpret_cast<f32x4*>(qs + wave * 512 + 256 + 8 * c) = qa[2];
      *reinterpret_cast<f32x4*>(qs + wave * 512 + 256 + 8 * c + 4) = qa[3];
    }
    u32x4* wl = reinterpret_cast<u32x4*>(lds + L_WO);
#pragma unroll
    for (int i = 0; i < 7; ++i) wl[t + 256 * i] = wv[i];
    float* vec = lds + L_B;
    if (btask) {
      float g = 0.f, u = 0.f;
#pragma unroll
      for (int k8 = 0; k8 < KPART; ++k8) { g += gu[k8]; u += gu[KPART + k8]; }
      vec[t] = bfr(bfr(gelu_tanh(bfr(g))) * bfr(u));
    }
    __syncthreads();
#pragma unroll
    for (int u = 0; u < 4; ++u) {
      const float* qp = qs + (u >> 1) * 256 + 8 * c + (u & 1) * 4;
      qa[u] = (*reinterpret_cast<const f32x4*>(qp) + *reinterpret_cast<const f32x4*>(qp + 512)) +
              (*reinterpret_cast<const f32x4*>(qp + 1024) + *reinterpret_cast<const f32x4*>(qp + 1536));
    }
#pragma unroll
    for (int k = 0; k < 4; ++k) {
      q0[k] = (unsigned)f2bf(qa[k >> 1][(2 * k) & 3]) | ((unsigned)f2bf(qa[k >> 1][(2 * k + 1) & 3]) << 16);
      q1[k] = (unsigned)f2bf(qa[2 + (k >> 1)][(2 * k) & 3]) | ((unsigned)f2bf(qa[2 + (k >> 1)][(2 * k + 1) & 3]) << 16);
    }
    if (btask) {
      const int rg = t >> 3;
      float* red = lds + L_B + 256;
      float a2[2][8];
#pragma unroll
      for (int bb = 0; bb < 2; ++bb)
#pragma unroll
        for (int k = 0; k < 8; ++k) a2[bb][k] = 0.f;
#pragma unroll
      for (int i = 0; i < 4; ++i) {
        const float x0 = vec[rg + 32 * i], x1 = vec[128 + rg + 32 * i];
#pragma unroll
        for (int k = 0; k < 4; ++k) {
          const float lo = bflo(wdt[i][k]), hi = bfhi(wdt[i][k]);
          a2[0][2 * k] = fmaf(x0, lo, a2[0][2 * k]); a2[0][2 * k + 1] = fmaf(x0, hi, a2[0][2 * k + 1]);
          a2[1][2 * k] = fmaf(x1, lo, a2[1][2 * k]); a2[1][2 * k + 1] = fmaf(x1, hi, a2[1][2 * k + 1]);
        }
      }
#pragma unroll
      for (int bb = 0; bb < 2; ++bb)
#pragma unroll
        for (int k = 0; k < 8; ++k) {
          float v = a2[bb][k];
          v += __shfl_xor(v, 8, 64); v += __shfl_xor(v, 16, 64); v += __shfl_xor(v, 32, 64);
          a2[bb][k] = v;
        }
      if (lane < 8) {
#pragma unroll
        for (int bb = 0; bb < 2; ++bb)
#pragma unroll
          for (int k = 0; k < 8; ++k) red[(wave * 2 + bb) * 64 + lane * 8 + k] = a2[bb][k];
      }
      __syncthreads();
      if (t < 128) {
        const int bb = t >> 6, cc = t & 63;
        const float v = (red[(0 * 2 + bb) * 64 + cc] + red[(1 * 2 + bb) * 64 + cc]) + (red[(2 * 2 + bb) * 64 + cc] + red[(3 * 2 + bb) * 64 + cc]);
        put_granule(G + G_OB + (size_t)(bkp * 2 + bb) * DM + bcg * 64 + cc, v);
      }
    }
  }
  __builtin_amdgcn_sched_barrier(0);
  float acc[16];
#pragma unroll
  for (int k = 0; k < 16; ++k) acc[k] = 0.f;
  float m = -INFINITY, l = 0.f;
  float* pw = plds + wave * 64;
  constexpr float SC = 0.0625f * 1.4426950408889634f;
  bool more;
  do {
    float xx[16];
#pragma unroll
    for (int i = 0; i < 16; ++i) {
      float s0 = 0.f, s1 = 0.f;
#pragma unroll
      for (int k = 0; k < 4; ++k) { s0 = dot2(kb[i][k], q0[k], s0); s1 = dot2(kb[i][k], q1[k], s1); }
      auto r = __builtin_amdgcn_permlane16_swap(__float_as_uint(s0), __float_as_uint(s1), false, false);
      xx[i] = __uint_as_float(r[0]) + __uint_as_float(r[1]);
    }
    PIN16(xx);
    more = cur < last;
    const unsigned von = (more ? (cur + 1) : (unsigned)NCHUNK_BH) * CHUNK_BYTES + lane * 16;
#pragma unroll
    for (int i = 0; i < 16; ++i) kb[i] = ldkv(kr, von + (i & 3) * 1024, (i >> 2) * 4096);
    asm volatile("" ::: "memory");
#define TR_STEP(HALF, CTRL, BIT)                                                       \
  _Pragma("unroll") for (int h = 0; h < (HALF); ++h) {                                 \
    const bool up = (lane & (BIT)) != 0;                                               \
    const float keep = up ? xx[h + (HALF)] : xx[h], send = up ? xx[h] : xx[h + (HALF)]; \
    xx[h] = keep + dpp<CTRL>(send);                                                    \
  }
    TR_STEP(8, XOR8, 8) TR_STEP(4, XOR7, 4) TR_STEP(2, XOR2, 2) TR_STEP(1, XOR1, 1)
#undef TR_STEP
    const float s = bfr(xx[0]) * SC;
    float pm = s;
    pm = fmaxf(pm, dpp<XOR1>(pm)); pm = fmaxf(pm, dpp<XOR2>(pm)); pm = fmaxf(pm, dpp<XOR7>(pm)); pm = fmaxf(pm, dpp<XOR8>(pm));
    {
      auto r = __builtin_amdgcn_permlane32_swap(__float_as_uint(pm), __float_as_uint(pm), false, false);
      pm = fmaxf(__uint_as_float(r[0]), __uint_as_float(r[1]));
    }
    const float mn = fmaxf(m, pm);
    const float alpha = __builtin_amdgcn_exp2f(m - mn);
    const float p = __builtin_amdgcn_exp2f(s - mn);
    l = fmaf(l, alpha, p);
    m = mn;
    const float a0 = __uint_as_float(__builtin_amdgcn_readlane(__float_as_uint(alpha), 0));
    const float a1 = __uint_as_float(__builtin_amdgcn_readlane(__float_as_uint(alpha), 16));
#pragma unroll
    for (int k = 0; k < 8; ++k) { acc[k] *= a0; acc[8 + k] *= a1; }
    pw[lane] = p;
    asm volatile("s_waitcnt lgkmcnt(0)" ::: "memory");
    const float* prd = pw + (lane >> 5) * 32;
#pragma unroll
    for (int u = 0; u < 4; ++u) {
      const f32x4 pa = *reinterpret_cast<const f32x4*>(prd + 4 * u);
      const f32x4 pb = *reinterpret_cast<const f32x4*>(prd + 16 + 4 * u);
      asm volatile("" : "+v"(vb[4 * u]), "+v"(vb[4 * u + 1]), "+v"(vb[4 * u + 2]), "+v"(vb[4 * u + 3]));
#pragma unroll
      for (int j = 0; j < 4; ++j) {
        const int i = 4 * u + j;
        const float p0 = pa[j], p1 = pb[j];
#pragma unroll
        for (int k = 0; k < 4; ++k) {
          const float lo = bflo(vb[i][k]), hi = bfhi(vb[i][k]);
          acc[2 * k] = fmaf(p0, lo, acc[2 * k]); acc[2 * k + 1] = fmaf(p0, hi, acc[2 * k + 1]);
          acc[8 + 2 * k] = fmaf(p1, lo, acc[8 + 2 * k]); acc[8 + 2 * k + 1] = fmaf(p1, hi, acc[8 + 2 * k + 1]);
        }
      }
    }
    PIN16(acc);
#pragma unroll
    for (int i = 0; i < 16; ++i) vb[i] = ldkv(vr, von + (i & 3) * 1024, (i >> 2) * 4096);
    asm volatile("" ::: "memory");
    ++cur;
  } while (more);
#pragma unroll
  for (int k = 0; k < 16; ++k) {
    auto r = __builtin_amdgcn_permlane32_swap(__float_as_uint(acc[k]), __float_as_uint(acc[k]), false, false);
    acc[k] = __uint_as_float(r[0]) + __uint_as_float(r[1]);
  }
  l += dpp<XOR1>(l); l += dpp<XOR2>(l); l += dpp<XOR7>(l); l += dpp<XOR8>(l);
  {
    auto r = __builtin_amdgcn_permlane32_swap(__float_as_uint(l), __float_as_uint(l), false, false);
    l = __uint_as_float(r[0]) + __uint_as_float(r[1]);
  }
  if (lane < 32) {
#pragma unroll
    for (int h = 0; h < 2; ++h) {
      *reinterpret_cast<f32x4*>(mrg + (wave * 2 + h) * DH + 8 * c) = f32x4{acc[8 * h + 0], acc[8 * h + 1], acc[8 * h + 2], acc[8 * h + 3]};
      *reinterpret_cast<f32x4*>(mrg + (wave * 2 + h) * DH + 8 * c + 4) = f32x4{acc[8 * h + 4], acc[8 * h + 5], acc[8 * h + 6], acc[8 * h + 7]};
    }
    if ((lane & 15) == 0) { const int h = lane >> 4; mls[(wave * 2 + h) * 2] = m; mls[(wave * 2 + h) * 2 + 1] = l; }
  }
  __syncthreads();
#pragma unroll
  for (int h = 0; h < 2; ++h) {
    float M = -INFINITY;
#pragma unroll
    for (int w = 0; w < ATT_WAVES; ++w) M = fmaxf(M, mls[(w * 2 + h) * 2]);
    float L = 0.f, o = 0.f;
#pragma unroll
    for (int w = 0; w < ATT_WAVES; ++w) {
      const float wt = __builtin_amdgcn_exp2f(mls[(w * 2 + h) * 2] - M);
      L = fmaf(wt, mls[(w * 2 + h) * 2 + 1], L);
      o = fmaf(wt, mrg[(w * 2 + h) * DH + t], o);
    }
    put_granule(G + G_PO + (size_t)blockIdx.x * 512 + h * 256 + t, o);
    if (t == 0) { put_granule(G + G_ST + (size_t)blockIdx.x * 4 + h * 2, M); put_granule(G + G_ST + (size_t)blockIdx.x * 4 + h * 2 + 1, L); }
  }
  float* gM = lds + L_G;
  float* gL = gM + 16;
  float* gO = gM + 32;
  float* ov = gO + 512;
  {
    const int hh = split >> 3, d0 = (split & 7) * 32;
    const int s = t >> 4, j = t & 15;
    u64* gp = G + G_PO + (size_t)(bh * NSPLIT + s) * 512 + hh * 256 + d0 + 2 * j;
    u64* gs = G + G_ST + (size_t)(bh * NSPLIT + s) * 4 + hh * 2;
    u64 g0, g1, g2, g3;
    for (unsigned spin = 0; spin < SPIN_CAP; ++spin) {
      g0 = get_granule(gp); g1 = get_granule(gp + 1); g2 = get_granule(gs); g3 = get_granule(gs + 1);
      const bool ok = ((g0 & g1 & g2 & g3) >> 32) == 1ull;
      if (__all(ok)) break;
      __builtin_amdgcn_s_sleep(2);
    }
    gO[s * 32 + 2 * j] = __uint_as_float((unsigned)g0);
    gO[s * 32 + 2 * j + 1] = __uint_as_float((unsigned)g1);
    if (j == 0) { gM[s] = __uint_as_float((unsigned)g2); gL[s] = __uint_as_float((unsigned)g3); }
  }
  __syncthreads();
  if (t < 32) {
    float M = -INFINITY;
#pragma unroll
    for (int s = 0; s < NSPLIT; ++s) M = fmaxf(M, gM[s]);
    float L = 0.f, o = 0.f;
#pragma unroll
    for (int s = 0; s < NSPLIT; ++s) {
      const float wt = __builtin_amdgcn_exp2f(gM[s] - M);
      L = fmaf(wt, gL[s], L); o = fmaf(wt, gO[s * 32 + t], o);
    }
    ov[t] = bfr(o / L);
  }
  __syncthreads();
  if (t < DM / 2) {
    const unsigned* wl = reinterpret_cast<const unsigned*>(lds + L_WO) + t;
    float y0 = 0.f, y1 = 0.f;
#pragma unroll
    for (int d = 0; d < 32; ++d) {
      const unsigned w = wl[d * (DM / 2)];
      const float od = ov[d];
      y0 = fmaf(od, bflo(w), y0); y1 = fmaf(od, bfhi(w), y1);
    }
    u64* g = G + G_OA + (size_t)blockIdx.x * DM + 2 * t;
    put_granule(g, y0); put_granule(g + 1, y1);
  }
  if (blockIdx.x < NRED) {
    const int br = blockIdx.x / 28, c0 = (blockIdx.x % 28) * 16;
    const int col = t & 15, rgp = t >> 4;
    u64* ga = G + G_OA + (size_t)(br * 128 + rgp * 8) * DM + c0 + col;
    u64* gb = G + G_OB + (size_t)((rgp < KP_D ? rgp : 0) * 2 + br) * DM + c0 + col;
    u64 va[8], vbq;
    for (unsigned spin = 0; spin < SPIN_CAP; ++spin) {
      u64 all = ~0ull;
#pragma unroll
      for (int i = 0; i < 8; ++i) { va[i] = get_granule(ga + (size_t)i * DM); all &= va[i]; }
      vbq = get_granule(gb); all &= vbq;
      if (__all((all >> 32) == 1ull)) break;
      __builtin_amdgcn_s_sleep(2);
    }
    float A = 0.f;
#pragma unroll
    for (int i = 0; i < 8; ++i) A += __uint_as_float((unsigned)va[i]);
    float* ra = mrg;
    float* rb = mrg + 256;
    ra[rgp * 16 + col] = A;
    rb[rgp * 16 + col] = rgp < KP_D ? __uint_as_float((unsigned)vbq) : 0.f;
    __syncthreads();
    if (t < 16) {
      float As = 0.f, Bs = 0.f;
#pragma unroll
      for (int r = 0; r < 16; ++r) { As += ra[r * 16 + t]; Bs += rb[r * 16 + t]; }
      const float xv = bf2f(x[br * DM + c0 + t]);
      out[br * DM + c0 + t] = f2bf(bfr(xv + bfr(As)) + bfr(Bs));
    }
  }
}

extern "C" void kernel_launch(void* const* d_in, const int* in_sizes, int n_in, void* d_out, int out_size, void* d_ws,
                              size_t ws_size, hipStream_t stream) {
  const unsigned short* x = (const unsigned short*)d_in[0];
  const unsigned short* K = (const unsigned short*)d_in[1];
  const unsigned short* V = (const unsigned short*)d_in[2];
  const unsigned short* wq = (const unsigned short*)d_in[3];
  const unsigned short* wo = (const unsigned short*)d_in[4];
  const unsigned short* wg = (const unsigned short*)d_in[6];
  const unsigned short* wu = (const unsigned short*)d_in[7];
  const unsigned short* wd = (const unsigned short*)d_in[8];
  unsigned char* ws = (unsigned char*)d_ws;
  prep_kernel<<<PREP_WGS, 256, 0, stream>>>(x, wq, wg, wu, ws);
  attn_kernel<<<NWG, ATT_THREADS, 0, stream>>>(K, V, x, wo, wd, ws, (unsigned short*)d_out);
}
```
